```python
import torch

def _softmax(x, axis=-1):
    return torch.softmax(x, dim=axis)
def _gelu(x):
    return torch.nn.functional.gelu(x, approximate="tanh")
def _silu(x):
    return torch.nn.functional.silu(x)
def _sigmoid(x):
    return torch.sigmoid(x)
def _layernorm(x, axis=-1, eps=1e-5):
    m = x.mean(dim=axis, keepdim=True)
    v = x.var(dim=axis, keepdim=True, unbiased=False)
    return (x - m) / torch.sqrt(v + eps)
def _scatter_add(tgt, idx, src, axis=0):
    return tgt.clone().index_add_(axis, idx, src)
def _segment_sum(src, seg, n):
    out = torch.zeros((n,) + src.shape[1:], dtype=src.dtype)
    return out.index_add_(0, seg, src)
def _pad(x, pw):
    flat = tuple(v for pair in reversed(pw) for v in pair)
    return torch.nn.functional.pad(x, flat)

kernel_name = 'recurrent_scan__sliding_local_dilated__glu_gated_swiglu__prompt'

def setup_inputs():

    _w = (lambda a: (lambda t: t.to(torch.bfloat16) if t.is_floating_point() else t)(torch.as_tensor(a)))
    def _A_setup():
        B, S, D = 1, 2048, 1024
        return {
            "x": _w((lambda *s: torch.randn(*s, dtype=torch.bfloat16) * 0.05)(B, S, D)),
            "wqkv": _w((lambda *s: torch.randn(*s, dtype=torch.bfloat16) * 0.2)(D, 3 * D)),
            "wo": _w((lambda *s: torch.randn(*s, dtype=torch.bfloat16) * 0.2)(D, D)),
        }
    _A_in = _A_setup()

    def _B_setup():
        B, S, D, F = 1, 2048, 1024, 3584
        return {
            "x": _w((lambda *s: torch.randn(*s, dtype=torch.bfloat16) * 0.05)(B, S, D)),
            "w1": _w((lambda *s: torch.randn(*s, dtype=torch.bfloat16) * 0.1)(D, F) * (2.0 / D) ** 0.5),
            "w3": _w((lambda *s: torch.randn(*s, dtype=torch.bfloat16) * 0.1)(D, F) * (2.0 / D) ** 0.5),
            "w2": _w((lambda *s: torch.randn(*s, dtype=torch.bfloat16) * 0.1)(F, D) * (2.0 / F) ** 0.5),
        }
    _B_in = _B_setup()

    return {
        "A_x": _A_in["x"],
        "A_wqkv": _A_in["wqkv"],
        "A_wo": _A_in["wo"],
        "B_x": _B_in["x"],
        "B_w1": _B_in["w1"],
        "B_w3": _B_in["w3"],
        "B_w2": _B_in["w2"],
    }

def reference(A_x, A_wqkv, A_wo, B_x, B_w1, B_w3, B_w2):
    def _A_ref(x, wqkv, wo):
        B, S, D = x.shape
        H = 32; Dh = D // H
        W, dil = 8, 2
        qkv = (lambda x, *d: x.permute(*d))((x @ wqkv).reshape(B, S, 3, H, Dh), 2, 0, 3, 1, 4)
        q, k, v = qkv[0], qkv[1], qkv[2]
        scale = Dh ** -0.5
        pos = torch.arange(S)
        off = torch.arange(W) * dil
        idx = pos[:, None] - off[None, :]
        mask = idx >= 0
        idx_c = torch.where(mask, idx, 0).reshape(-1)
        kg = k[:, :, idx_c, :].reshape(B, H, S, W, Dh)
        vg = v[:, :, idx_c, :].reshape(B, H, S, W, Dh)
        logits = torch.einsum("bhsd,bhswd->bhsw", q, kg) * scale
        logits = torch.where(mask[None, None, :, :], logits, -1e9)
        attn = _softmax(logits, axis=-1)
        o = torch.einsum("bhsw,bhswd->bhsd", attn, vg)
        return (lambda x, *d: x.permute(*d))(o, 0, 2, 1, 3).reshape(B, S, D) @ wo

    def _B_ref(x, w1, w3, w2):
        gate = torch.einsum("bsd,df->bsf", x, w1)
        up = torch.einsum("bsd,df->bsf", x, w3)
        h = _silu(gate) * up
        out = torch.einsum("bsf,fd->bsd", h, w2)
        return out

    x = A_x
    xn = _layernorm(x, axis=-1)
    s = _A_ref(xn, A_wqkv, A_wo)
    g = _silu(_B_ref(xn, B_w1, B_w3, B_w2))
    return x + s * g
```

```cpp
#include <hip/hip_runtime.h>
#include <hip/hip_bf16.h>
#include <stdint.h>

typedef __attribute__((ext_vector_type(8))) short bf16x8;
typedef __attribute__((ext_vector_type(4))) short s16x4;
typedef __attribute__((ext_vector_type(4))) float f32x4;
typedef unsigned short u16;
typedef __attribute__((address_space(3))) s16x4* lds_s16x4_p;

constexpr int S_ = 2048, D_ = 1024, F_ = 3584, NH = 32, DH = 32;

__device__ __forceinline__ u16 f2bf(float f) {
  uint32_t u = __float_as_uint(f);
  u += 0x7fffu + ((u >> 16) & 1u);
  return (u16)(u >> 16);
}
__device__ __forceinline__ float bf2f(u16 h) { return __uint_as_float(((uint32_t)h) << 16); }
__device__ __forceinline__ float rbf(float f) { return bf2f(f2bf(f)); }
__device__ __forceinline__ float silu_f(float v) { return v / (1.0f + __expf(-v)); }

__device__ __forceinline__ void glds16(const void* g, void* l) {
  __builtin_amdgcn_global_load_lds((const __attribute__((address_space(1))) unsigned*)g,
                                   (__attribute__((address_space(3))) unsigned*)l, 16, 0, 0);
}
__device__ __forceinline__ bf16x8 lds_read128(const char* p) {
  return *reinterpret_cast<const bf16x8*>(p);
}
__device__ __forceinline__ bf16x8 lds_read_tr(const char* p0, const char* p1) {
  s16x4 a = __builtin_amdgcn_ds_read_tr16_b64_v4i16((lds_s16x4_p)p0);
  s16x4 b = __builtin_amdgcn_ds_read_tr16_b64_v4i16((lds_s16x4_p)p1);
  bf16x8 r;
  r[0] = a[0]; r[1] = a[1]; r[2] = a[2]; r[3] = a[3];
  r[4] = b[0]; r[5] = b[1]; r[6] = b[2]; r[7] = b[3];
  return r;
}

__global__ __launch_bounds__(256) void ln_kernel(const u16* __restrict__ x, u16* __restrict__ xn) {
  const int lane = threadIdx.x & 63;
  const int row = blockIdx.x * 4 + (threadIdx.x >> 6);
  const u16* xr = x + (size_t)row * D_;
  bf16x8 v0 = *reinterpret_cast<const bf16x8*>(xr + lane * 8);
  bf16x8 v1 = *reinterpret_cast<const bf16x8*>(xr + 512 + lane * 8);
  float f[16];
#pragma unroll
  for (int i = 0; i < 8; ++i) { f[i] = bf2f((u16)v0[i]); f[8 + i] = bf2f((u16)v1[i]); }
  float s = 0.f;
#pragma unroll
  for (int i = 0; i < 16; ++i) s += f[i];
#pragma unroll
  for (int o = 32; o >= 1; o >>= 1) s += __shfl_xor(s, o);
  const float mean = s * (1.0f / D_);
  float q = 0.f;
#pragma unroll
  for (int i = 0; i < 16; ++i) { float d = f[i] - mean; q += d * d; }
#pragma unroll
  for (int o = 32; o >= 1; o >>= 1) q += __shfl_xor(q, o);
  const float var = q * (1.0f / D_);
  const float mb = rbf(mean);
  const float vb = rbf(var);
  const float den = rbf(sqrtf(rbf(vb + 1e-5f)));
  bf16x8 o0, o1;
#pragma unroll
  for (int i = 0; i < 8; ++i) {
    o0[i] = (short)f2bf(rbf(f[i] - mb) / den);
    o1[i] = (short)f2bf(rbf(f[8 + i] - mb) / den);
  }
  u16* orow = xn + (size_t)row * D_;
  *reinterpret_cast<bf16x8*>(orow + lane * 8) = o0;
  *reinterpret_cast<bf16x8*>(orow + 512 + lane * 8) = o1;
}

__device__ __forceinline__ void g1_nt_map(int nt, int& type, int& idx) {
  const int w = nt / 5, r = nt - w * 5;
  if (w < 3) {
    if (r < 4) { type = r & 1; idx = w * 2 + (r >> 1); }
    else { type = 2; idx = w; }
  } else {
    if (r < 2) { type = r; idx = 6; }
    else { type = 2; idx = 1 + r; }
  }
}

constexpr int G1_ASZ = 256 * 128;
constexpr int G1_BSZ = 5 * 8192;
constexpr int G1_STAGE = G1_ASZ + G1_BSZ;

__global__ __launch_bounds__(512, 2) void gemm1_kernel(const u16* __restrict__ xn, const u16* __restrict__ wqkv,
                                                       const u16* __restrict__ w1, const u16* __restrict__ w3,
                                                       u16* __restrict__ qkv, u16* __restrict__ h) {
  __shared__ __attribute__((aligned(16))) char smem[2 * G1_STAGE];
  const int tid = threadIdx.x, lane = tid & 63, wid = tid >> 6;
  const int wr = wid >> 2, wc = wid & 3;
  const int fr = lane & 15, fq = lane >> 4;
  const int xcd = blockIdx.x & 7, idx = blockIdx.x >> 3;
  const int cb = xcd * 4 + (idx & 3), tm = idx >> 2;
  const int row0 = tm * 256;

  const u16* asrc[4];
#pragma unroll
  for (int j = 0; j < 4; ++j) {
    const int r = wid * 32 + j * 8 + (lane >> 3);
    const int c = (lane & 7) ^ ((r >> 1) & 7);
    asrc[j] = xn + (size_t)(row0 + r) * D_ + c * 8;
  }
  const u16* bsrc[5];
  int bld[5];
  {
    const int k = wid * 8 + (lane >> 3);
    const int p16 = lane & 7;
    const int g = ((k >> 1) & 1) | (((k >> 3) & 1) << 1);
    const int c = (p16 >> 1) ^ g, half = p16 & 1;
#pragma unroll
    for (int s = 0; s < 5; ++s) {
      int type, ix;
      g1_nt_map(s * 4 + c, type, ix);
      const u16* base = type == 0 ? w1 : (type == 1 ? w3 : wqkv);
      const int ld = type == 2 ? 3072 : F_;
      const int col = (type == 2 ? cb * 96 : cb * 112) + ix * 16 + half * 8;
      bsrc[s] = base + (size_t)k * ld + col;
      bld[s] = ld;
    }
  }
  auto stage = [&](int kt, int buf) {
    char* sa = smem + buf * G1_STAGE;
    char* sb = sa + G1_ASZ;
#pragma unroll
    for (int j = 0; j < 4; ++j)
      glds16(asrc[j] + kt * 64, sa + (wid * 32 + j * 8) * 128 + lane * 16);
#pragma unroll
    for (int s = 0; s < 5; ++s)
      glds16(bsrc[s] + (size_t)kt * 64 * bld[s], sb + s * 8192 + wid * 1024 + lane * 16);
  };

  f32x4 acc[8][5];
#pragma unroll
  for (int m = 0; m < 8; ++m)
#pragma unroll
    for (int n = 0; n < 5; ++n) acc[m][n] = (f32x4){0.f, 0.f, 0.f, 0.f};

  const int aswz = (fr >> 1) & 7;
  const int q4 = (lane & 15) >> 2, p4 = lane & 3;
  const int bswz = (q4 >> 1) | ((fq & 1) << 1);

  stage(0, 0);
  asm volatile("s_waitcnt vmcnt(0)" ::: "memory");
  __syncthreads();

  for (int kt = 0; kt < 16; ++kt) {
    const int buf = kt & 1;
    if (kt + 1 < 16) stage(kt + 1, buf ^ 1);
    const char* sa = smem + buf * G1_STAGE;
    const char* sb = sa + G1_ASZ;
#pragma unroll
    for (int kk = 0; kk < 2; ++kk) {
      bf16x8 a[8], b[5];
#pragma unroll
      for (int m = 0; m < 8; ++m)
        a[m] = lds_read128(sa + (wr * 128 + m * 16 + fr) * 128 + (((kk * 4 + fq) ^ aswz) * 16));
#pragma unroll
      for (int n = 0; n < 5; ++n) {
        const int nt = wc * 5 + n;
        const char* p = sb + (nt >> 2) * 8192 + (kk * 32 + fq * 8 + q4) * 128 + (((nt & 3) ^ bswz) * 32) + p4 * 8;
        b[n] = lds_read_tr(p, p + 512);
      }
#pragma unroll
      for (int m = 0; m < 8; ++m)
#pragma unroll
        for (int n = 0; n < 5; ++n)
          acc[m][n] = __builtin_amdgcn_mfma_f32_16x16x32_bf16(b[n], a[m], acc[m][n], 0, 0, 0);
    }
    asm volatile("s_waitcnt vmcnt(0)" ::: "memory");
    __syncthreads();
  }

#pragma unroll
  for (int n = 0; n < 5; ++n) {
    int type, ix;
    g1_nt_map(wc * 5 + n, type, ix);
    if (type == 2) {
      const int col = cb * 96 + ix * 16 + fq * 4;
#pragma unroll
      for (int m = 0; m < 8; ++m) {
        const int row = row0 + wr * 128 + m * 16 + fr;
        s16x4 o;
#pragma unroll
        for (int r = 0; r < 4; ++r) o[r] = (short)f2bf(acc[m][n][r]);
        *reinterpret_cast<s16x4*>(qkv + (size_t)row * 3072 + col) = o;
      }
    } else if (type == 0) {
      const int col = cb * 112 + ix * 16 + fq * 4;
#pragma unroll
      for (int m = 0; m < 8; ++m) {
        const int row = row0 + wr * 128 + m * 16 + fr;
        s16x4 o;
#pragma unroll
        for (int r = 0; r < 4; ++r) {
          const float g = rbf(acc[m][n][r]);
          const float u = rbf(acc[m][(n + 1) < 5 ? n + 1 : n][r]);
          o[r] = (short)f2bf(rbf(silu_f(g)) * u);
        }
        *reinterpret_cast<s16x4*>(h + (size_t)row * F_ + col) = o;
      }
    }
  }
}

__global__ __launch_bounds__(256) void attn_kernel(const u16* __restrict__ qkv, u16* __restrict__ o) {
  const int idx = blockIdx.x * 256 + threadIdx.x;
  const int c = idx & 3, hh = (idx >> 2) & 31, s = idx >> 7;
  const u16* base = qkv + hh * 32 + c * 8;
  bf16x8 qv = *reinterpret_cast<const bf16x8*>(base + (size_t)s * 3072);
  float qf[8];
#pragma unroll
  for (int i = 0; i < 8; ++i) qf[i] = bf2f((u16)qv[i]);
  float lg[8];
  const float scale = 0.17677669529663687f;
#pragma unroll
  for (int j = 0; j < 8; ++j) {
    const int pos = s - 2 * j;
    float d = 0.f;
    if (pos >= 0) {
      bf16x8 kv = *reinterpret_cast<const bf16x8*>(base + (size_t)pos * 3072 + 1024);
#pragma unroll
      for (int i = 0; i < 8; ++i) d += qf[i] * bf2f((u16)kv[i]);
    }
    d += __shfl_xor(d, 1);
    d += __shfl_xor(d, 2);
    lg[j] = (pos >= 0) ? rbf(rbf(d) * scale) : -1e9f;
  }
  float mx = lg[0];
#pragma unroll
  for (int j = 1; j < 8; ++j) mx = fmaxf(mx, lg[j]);
  float e[8], sum = 0.f;
#pragma unroll
  for (int j = 0; j < 8; ++j) { e[j] = __expf(lg[j] - mx); sum += e[j]; }
  const float inv = 1.0f / sum;
  float of[8];
#pragma unroll
  for (int i = 0; i < 8; ++i) of[i] = 0.f;
#pragma unroll
  for (int j = 0; j < 8; ++j) {
    const int pos = s - 2 * j;
    if (pos >= 0) {
      const float a = rbf(e[j] * inv);
      bf16x8 vv = *reinterpret_cast<const bf16x8*>(base + (size_t)pos * 3072 + 2048);
#pragma unroll
      for (int i = 0; i < 8; ++i) of[i] += a * bf2f((u16)vv[i]);
    }
  }
  bf16x8 ov;
#pragma unroll
  for (int i = 0; i < 8; ++i) ov[i] = (short)f2bf(of[i]);
  *reinterpret_cast<bf16x8*>(o + (size_t)s * D_ + hh * 32 + c * 8) = ov;
}

constexpr int G2_ASZ = 128 * 128;
constexpr int G2_BSZ = 2 * 8192;
constexpr int G2_STAGE = G2_ASZ + G2_BSZ;

__device__ __forceinline__ void gemm2_loop(const u16* __restrict__ A, int lda, const u16* __restrict__ B,
                                           int row0, int col0, int k0, int nkt, char* smem, f32x4 (&acc)[4][4]) {
  const int tid = threadIdx.x, lane = tid & 63, wid = tid >> 6;
  const int wr = wid >> 1, wc = wid & 1;
  const int fr = lane & 15, fq = lane >> 4;
  const u16* asrc[4];
#pragma unroll
  for (int j = 0; j < 4; ++j) {
    const int r = wid * 32 + j * 8 + (lane >> 3);
    const int c = (lane & 7) ^ ((r >> 1) & 7);
    asrc[j] = A + (size_t)(row0 + r) * lda + k0 + c * 8;
  }
  const u16* bsrc[2][2];
#pragma unroll
  for (int j = 0; j < 2; ++j) {
    const int k = wid * 16 + j * 8 + (lane >> 3);
    const int p16 = lane & 7;
    const int g = ((k >> 1) & 1) | (((k >> 3) & 1) << 1);
    const int c = (p16 >> 1) ^ g, half = p16 & 1;
#pragma unroll
    for (int s = 0; s < 2; ++s) bsrc[s][j] = B + (size_t)(k0 + k) * D_ + col0 + s * 64 + c * 16 + half * 8;
  }
  auto stage = [&](int kt, int buf) {
    char* sa = smem + buf * G2_STAGE;
    char* sb = sa + G2_ASZ;
#pragma unroll
    for (int j = 0; j < 4; ++j) glds16(asrc[j] + kt * 64, sa + (wid * 32 + j * 8) * 128 + lane * 16);
#pragma unroll
    for (int s = 0; s < 2; ++s)
#pragma unroll
      for (int j = 0; j < 2; ++j)
        glds16(bsrc[s][j] + (size_t)kt * 64 * D_, sb + s * 8192 + (wid * 16 + j * 8) * 128 + lane * 16);
  };
  const int aswz = (fr >> 1) & 7;
  const int q4 = (lane & 15) >> 2, p4 = lane & 3;
  const int bswz = (q4 >> 1) | ((fq & 1) << 1);

  stage(0, 0);
  asm volatile("s_waitcnt vmcnt(0)" ::: "memory");
  __syncthreads();
  for (int kt = 0; kt < nkt; ++kt) {
    const int buf = kt & 1;
    if (kt + 1 < nkt) stage(kt + 1, buf ^ 1);
    const char* sa = smem + buf * G2_STAGE;
    const char* sb = sa + G2_ASZ;
#pragma unroll
    for (int kk = 0; kk < 2; ++kk) {
      bf16x8 a[4], b[4];
#pragma unroll
      for (int m = 0; m < 4; ++m)
        a[m] = lds_read128(sa + (wr * 64 + m * 16 + fr) * 128 + (((kk * 4 + fq) ^ aswz) * 16));
#pragma unroll
      for (int n = 0; n < 4; ++n) {
        const int nt = wc * 4 + n;
        const char* p = sb + (nt >> 2) * 8192 + (kk * 32 + fq * 8 + q4) * 128 + (((nt & 3) ^ bswz) * 32) + p4 * 8;
        b[n] = lds_read_tr(p, p + 512);
      }
#pragma unroll
      for (int m = 0; m < 4; ++m)
#pragma unroll
        for (int n = 0; n < 4; ++n)
          acc[m][n] = __builtin_amdgcn_mfma_f32_16x16x32_bf16(b[n], a[m], acc[m][n], 0, 0, 0);
    }
    asm volatile("s_waitcnt vmcnt(0)" ::: "memory");
    __syncthreads();
  }
}

__global__ __launch_bounds__(256, 1) void gemm2_kernel(const u16* __restrict__ o, const u16* __restrict__ wo,
                                                       const u16* __restrict__ h, const u16* __restrict__ w2,
                                                       u16* __restrict__ sbuf, float* __restrict__ g0,
                                                       float* __restrict__ g1) {
  __shared__ __attribute__((aligned(16))) char smem[2 * G2_STAGE];
  const int tid = threadIdx.x, lane = tid & 63, wid = tid >> 6;
  const int wr = wid >> 1, wc = wid & 1;
  const int fr = lane & 15, fq = lane >> 4;
  const int xcd = blockIdx.x & 7, idx = blockIdx.x >> 3;
  const int split = idx & 1;
  const int t = idx >> 1;
  const int tn = xcd;
  const int tm = t;
  const int row0 = tm * 128, col0 = tn * 128;
  f32x4 acc[4][4];
#pragma unroll
  for (int m = 0; m < 4; ++m)
#pragma unroll
    for (int n = 0; n < 4; ++n) acc[m][n] = (f32x4){0.f, 0.f, 0.f, 0.f};
  if (split == 0) {
    gemm2_loop(o, D_, wo, row0, col0, 0, 16, smem, acc);
#pragma unroll
    for (int m = 0; m < 4; ++m)
#pragma unroll
      for (int n = 0; n < 4; ++n) {
        const int row = row0 + wr * 64 + m * 16 + fr, col = col0 + wc * 64 + n * 16 + fq * 4;
        s16x4 ov;
#pragma unroll
        for (int r = 0; r < 4; ++r) ov[r] = (short)f2bf(acc[m][n][r]);
        *reinterpret_cast<s16x4*>(sbuf + (size_t)row * D_ + col) = ov;
        acc[m][n] = (f32x4){0.f, 0.f, 0.f, 0.f};
      }
    gemm2_loop(h, F_, w2, row0, col0, 0, 20, smem, acc);
  } else {
    gemm2_loop(h, F_, w2, row0, col0, 1280, 36, smem, acc);
  }
  float* gd = split == 0 ? g0 : g1;
#pragma unroll
  for (int m = 0; m < 4; ++m)
#pragma unroll
    for (int n = 0; n < 4; ++n) {
      const int row = row0 + wr * 64 + m * 16 + fr, col = col0 + wc * 64 + n * 16 + fq * 4;
      *reinterpret_cast<f32x4*>(gd + (size_t)row * D_ + col) = acc[m][n];
    }
}

__global__ __launch_bounds__(256) void final_kernel(const u16* __restrict__ x, const u16* __restrict__ sbuf,
                                                    const float* __restrict__ g0, const float* __restrict__ g1,
                                                    u16* __restrict__ out) {
  const size_t i = ((size_t)blockIdx.x * 256 + threadIdx.x) * 8;
  bf16x8 xv = *reinterpret_cast<const bf16x8*>(x + i);
  bf16x8 sv = *reinterpret_cast<const bf16x8*>(sbuf + i);
  f32x4 a0 = *reinterpret_cast<const f32x4*>(g0 + i), a1 = *reinterpret_cast<const f32x4*>(g0 + i + 4);
  f32x4 b0 = *reinterpret_cast<const f32x4*>(g1 + i), b1 = *reinterpret_cast<const f32x4*>(g1 + i + 4);
  bf16x8 ov;
#pragma unroll
  for (int k = 0; k < 8; ++k) {
    const float g = rbf(k < 4 ? a0[k] + b0[k] : a1[k - 4] + b1[k - 4]);
    const float sg = rbf(silu_f(g));
    const float p = rbf(bf2f((u16)sv[k]) * sg);
    ov[k] = (short)f2bf(bf2f((u16)xv[k]) + p);
  }
  *reinterpret_cast<bf16x8*>(out + i) = ov;
}

extern "C" void kernel_launch(void* const* d_in, const int* in_sizes, int n_in, void* d_out, int out_size,
                              void* d_ws, size_t ws_size, hipStream_t stream) {
  const u16* x = (const u16*)d_in[0];
  const u16* wqkv = (const u16*)d_in[1];
  const u16* wo = (const u16*)d_in[2];
  const u16* w1 = (const u16*)d_in[4];
  const u16* w3 = (const u16*)d_in[5];
  const u16* w2 = (const u16*)d_in[6];
  u16* out = (u16*)d_out;
  char* ws = (char*)d_ws;
  u16* xn = (u16*)ws;
  u16* qkv = (u16*)(ws + (4u << 20));
  u16* h = (u16*)(ws + (16u << 20));
  u16* o = (u16*)(ws + (32u << 20));
  u16* sbuf = (u16*)(ws + (36u << 20));
  float* g0 = (float*)(ws + (40u << 20));
  float* g1 = (float*)(ws + (48u << 20));

  ln_kernel<<<S_ / 4, 256, 0, stream>>>(x, xn);
  gemm1_kernel<<<256, 512, 0, stream>>>(xn, wqkv, w1, w3, qkv, h);
  attn_kernel<<<S_ * NH * 4 / 256, 256, 0, stream>>>(qkv, o);
  gemm2_kernel<<<256, 256, 0, stream>>>(o, wo, h, w2, sbuf, g0, g1);
  final_kernel<<<S_ * D_ / 8 / 256, 256, 0, stream>>>(x, sbuf, g0, g1, out);
}
```

```cpp
#include <hip/hip_runtime.h>
#include <hip/hip_bf16.h>
#include <stdint.h>
#include <type_traits>

typedef __attribute__((ext_vector_type(8))) short bf16x8;
typedef __attribute__((ext_vector_type(4))) short s16x4;
typedef __attribute__((ext_vector_type(4))) float f32x4;
typedef unsigned short u16;
typedef __attribute__((address_space(3))) s16x4* lds_s16x4_p;

constexpr int S_ = 2048, D_ = 1024, F_ = 3584, NH = 32, DH = 32;

__device__ __forceinline__ u16 f2bf(float f) {
  uint32_t u = __float_as_uint(f);
  u += 0x7fffu + ((u >> 16) & 1u);
  return (u16)(u >> 16);
}
__device__ __forceinline__ float bf2f(u16 h) { return __uint_as_float(((uint32_t)h) << 16); }
__device__ __forceinline__ float rbf(float f) { return bf2f(f2bf(f)); }
__device__ __forceinline__ float silu_f(float v) { return v / (1.0f + __expf(-v)); }

__device__ __forceinline__ void glds16(const void* g, void* l) {
  __builtin_amdgcn_global_load_lds((const __attribute__((address_space(1))) unsigned*)g,
                                   (__attribute__((address_space(3))) unsigned*)l, 16, 0, 0);
}
__device__ __forceinline__ bf16x8 lds_read128(const char* p) {
  return *reinterpret_cast<const bf16x8*>(p);
}
template <int OFF>
__device__ __forceinline__ void lds_tr_issue(s16x4& a, s16x4& b, unsigned addr) {
  asm volatile("ds_read_b64_tr_b16 %0, %2 offset:%3\n\tds_read_b64_tr_b16 %1, %2 offset:%4"
               : "=&v"(a), "=&v"(b)
               : "v"(addr), "i"(OFF), "i"(OFF + 512)
               : "memory");
}
template <int OFF>
__device__ __forceinline__ void lds_b128_issue(bf16x8& a, unsigned addr) {
  asm volatile("ds_read_b128 %0, %1 offset:%2" : "=&v"(a) : "v"(addr), "i"(OFF) : "memory");
}
template <int I, int MT, int OFF>
__device__ __forceinline__ void lds_a_issue_rec(bf16x8 (&a)[MT], unsigned addr) {
  if constexpr (I < MT) {
    lds_b128_issue<OFF + I * 2048>(a[I], addr);
    lds_a_issue_rec<I + 1, MT, OFF>(a, addr);
  }
}
template <int MT, int OFF>
__device__ __forceinline__ void lds_a_issue(bf16x8 (&a)[MT], unsigned addr) { lds_a_issue_rec<0, MT, OFF>(a, addr); }
__device__ __forceinline__ void opaque(s16x4& v) { asm volatile("" : "+v"(v)); }
__device__ __forceinline__ void opaque(bf16x8& v) { asm volatile("" : "+v"(v)); }
template <int NT, int MT>
__device__ __forceinline__ void lds_wait_all(s16x4 (&bl)[NT], s16x4 (&bh)[NT], bf16x8 (&a)[MT]) {
  asm volatile("s_waitcnt lgkmcnt(0)" ::: "memory");
#pragma unroll
  for (int n = 0; n < NT; ++n) { opaque(bl[n]); opaque(bh[n]); }
#pragma unroll
  for (int m = 0; m < MT; ++m) opaque(a[m]);
}
__device__ __forceinline__ bf16x8 cat4(s16x4 a, s16x4 b) {
  return __builtin_shufflevector(a, b, 0, 1, 2, 3, 4, 5, 6, 7);
}
__device__ __forceinline__ unsigned lds_addr(const void* p) {
  return (unsigned)(uintptr_t)(const __attribute__((address_space(3))) char*)p;
}

__global__ __launch_bounds__(256) void ln_kernel(const u16* __restrict__ x, u16* __restrict__ xn) {
  const int lane = threadIdx.x & 63;
  const int row = blockIdx.x * 4 + (threadIdx.x >> 6);
  const u16* xr = x + (size_t)row * D_;
  bf16x8 v0 = *reinterpret_cast<const bf16x8*>(xr + lane * 8);
  bf16x8 v1 = *reinterpret_cast<const bf16x8*>(xr + 512 + lane * 8);
  float f[16];
#pragma unroll
  for (int i = 0; i < 8; ++i) { f[i] = bf2f((u16)v0[i]); f[8 + i] = bf2f((u16)v1[i]); }
  float s = 0.f;
#pragma unroll
  for (int i = 0; i < 16; ++i) s += f[i];
#pragma unroll
  for (int o = 32; o >= 1; o >>= 1) s += __shfl_xor(s, o);
  const float mean = s * (1.0f / D_);
  float q = 0.f;
#pragma unroll
  for (int i = 0; i < 16; ++i) { float d = f[i] - mean; q += d * d; }
#pragma unroll
  for (int o = 32; o >= 1; o >>= 1) q += __shfl_xor(q, o);
  const float var = q * (1.0f / D_);
  const float mb = rbf(mean);
  const float vb = rbf(var);
  const float den = rbf(sqrtf(rbf(vb + 1e-5f)));
  bf16x8 o0, o1;
#pragma unroll
  for (int i = 0; i < 8; ++i) {
    o0[i] = (short)f2bf(rbf(f[i] - mb) / den);
    o1[i] = (short)f2bf(rbf(f[8 + i] - mb) / den);
  }
  u16* orow = xn + (size_t)row * D_;
  *reinterpret_cast<bf16x8*>(orow + lane * 8) = o0;
  *reinterpret_cast<bf16x8*>(orow + 512 + lane * 8) = o1;
}

template <int BM, int BN, int WAVES_M, int WAVES_N>
struct GemmCfg {
  static constexpr int NWN = WAVES_N;
  static constexpr int WM = BM / WAVES_M, WN = BN / WAVES_N;
  static constexpr int MT = WM / 16, NT = WN / 16;
  static constexpr int ASZ = BM * 128, BSZ = (BN / 64) * 8192;
  static constexpr int LDS = 2 * (ASZ + BSZ);
  static constexpr int AI = BM / 64;
  static constexpr int BI = BN / 64;
};

template <class C>
__device__ __forceinline__ void gemm_mainloop(const u16* __restrict__ aptr, int lda, const u16* __restrict__ bptr,
                                              int bsub, int ldb, int nkt, char* smem,
                                              f32x4 (&acc)[C::MT][C::NT]) {
  const int tid = threadIdx.x, lane = tid & 63, wid = tid >> 6;
  const int wu = __builtin_amdgcn_readfirstlane(wid);
  const int wm = wu / C::NWN, wn = wu % C::NWN;
  const int fr = lane & 15, fq = lane >> 4;
  unsigned aoff[C::AI];
#pragma unroll
  for (int j = 0; j < C::AI; ++j) {
    const int r = wu * (C::AI * 8) + j * 8 + (lane >> 3);
    const int c = (lane & 7) ^ ((r >> 1) & 7);
    aoff[j] = (unsigned)(r * lda + c * 8) * 2u;
  }
  const char* abase = (const char*)aptr;
  auto stage = [&](int kt, int buf) {
    char* sa = smem + buf * C::ASZ + wu * (C::AI * 1024);
    char* sb = smem + 2 * C::ASZ + buf * C::BSZ + wu * 1024;
#pragma unroll
    for (int j = 0; j < C::AI; ++j) glds16(abase + kt * 128 + aoff[j], sa + j * 1024);
    const u16* bp = bptr + (size_t)kt * 64 * ldb;
#pragma unroll
    for (int s = 0; s < C::BI; ++s) glds16(bp + s * bsub, sb + s * 8192);
  };
  const int aswz = (fr >> 1) & 7;
  const int q4 = (lane & 15) >> 2, p4 = lane & 3;
  const int bswz = (q4 >> 1) | ((fq & 1) << 1);
  const unsigned sbase = lds_addr(smem);
  unsigned a_addr[2], b_addr[C::NT];
#pragma unroll
  for (int kk = 0; kk < 2; ++kk) a_addr[kk] = sbase + (wm * C::WM + fr) * 128 + (((kk * 4 + fq) ^ aswz) * 16);
#pragma unroll
  for (int n = 0; n < C::NT; ++n) {
    const int nt = wn * C::NT + n;
    b_addr[n] = sbase + 2 * C::ASZ + (nt >> 2) * 8192 + (fq * 8 + q4) * 128 + (((nt & 3) ^ bswz) * 32) + p4 * 8;
  }
  auto compute = [&](auto bufc) {
    constexpr int BUF = decltype(bufc)::value;
#pragma unroll
    for (int kk = 0; kk < 2; ++kk) {
      s16x4 bl[C::NT], bh[C::NT];
      bf16x8 a[C::MT];
#pragma unroll
      for (int n = 0; n < C::NT; ++n) {
        if (kk == 0) lds_tr_issue<BUF * C::BSZ>(bl[n], bh[n], b_addr[n]);
        else lds_tr_issue<BUF * C::BSZ + 4096>(bl[n], bh[n], b_addr[n]);
      }
      lds_a_issue<C::MT, BUF * C::ASZ>(a, a_addr[kk]);
      lds_wait_all<C::NT, C::MT>(bl, bh, a);
#pragma unroll
      for (int m = 0; m < C::MT; ++m)
#pragma unroll
        for (int n = 0; n < C::NT; ++n)
          acc[m][n] = __builtin_amdgcn_mfma_f32_16x16x32_bf16(cat4(bl[n], bh[n]), a[m], acc[m][n], 0, 0, 0);
    }
  };
  stage(0, 0);
  asm volatile("s_waitcnt vmcnt(0)" ::: "memory");
  __builtin_amdgcn_s_barrier();
  for (int kt = 0; kt < nkt; kt += 2) {
    stage(kt + 1, 1);
    compute(std::integral_constant<int, 0>{});
    asm volatile("s_waitcnt vmcnt(0)" ::: "memory");
    __builtin_amdgcn_s_barrier();
    if (kt + 2 < nkt) stage(kt + 2, 0);
    compute(std::integral_constant<int, 1>{});
    asm volatile("s_waitcnt vmcnt(0)" ::: "memory");
    __builtin_amdgcn_s_barrier();
  }
}

__device__ __forceinline__ int bchunk_col(int lane, int wid, int& krow) {
  krow = wid * 8 + (lane >> 3);
  const int p16 = lane & 7;
  const int g = ((krow >> 1) & 1) | (((krow >> 3) & 1) << 1);
  const int c = (p16 >> 1) ^ g, half = p16 & 1;
  return c * 16 + half * 8;
}

__device__ __forceinline__ void gemm256_8ph(const u16* __restrict__ aptr, int lda, const u16* __restrict__ bptr,
                                            int bsub, int ldb, int nt, char* smem, f32x4 (&acc)[2][2][4][2]) {
  const int tid = threadIdx.x, lane = tid & 63;
  const int wu = __builtin_amdgcn_readfirstlane(tid >> 6);
  const int wr = wu >> 2, wc = wu & 3;
  const int fr = lane & 15, fq = lane >> 4;
  constexpr int HT = 16384;
  unsigned aoff[2];
#pragma unroll
  for (int j = 0; j < 2; ++j) {
    const int r = wu * 16 + j * 8 + (lane >> 3);
    const int c = (lane & 7) ^ ((r >> 1) & 7);
    aoff[j] = (unsigned)(r * lda + c * 8) * 2u;
  }
  const char* abase = (const char*)aptr;
  const size_t ahalf = (size_t)128 * lda * 2;
#define SA_(b, h) (smem + ((b) * 2 + (h)) * HT)
#define SB_(b, h) (smem + 4 * HT + ((b) * 2 + (h)) * HT)
#define STAGE_A(b, h, kt)                                                              \
  do {                                                                                 \
    const char* _g = abase + (h) * ahalf + (size_t)(kt) * 128;                         \
    glds16(_g + aoff[0], SA_(b, h) + wu * 2048);                                       \
    glds16(_g + aoff[1], SA_(b, h) + wu * 2048 + 1024);                                \
  } while (0)
#define STAGE_B(b, h, kt)                                                              \
  do {                                                                                 \
    const u16* _g = bptr + (size_t)(kt) * 64 * ldb + (h) * 2 * bsub;                   \
    glds16(_g, SB_(b, h) + wu * 1024);                                                 \
    glds16(_g + bsub, SB_(b, h) + 8192 + wu * 1024);                                   \
  } while (0)
  const int aswz = (fr >> 1) & 7;
  const int q4 = (lane & 15) >> 2, p4 = lane & 3;
  const int bswz = (q4 >> 1) | ((fq & 1) << 1);
  const unsigned sbase = lds_addr(smem);
  unsigned a_addr[2], b_addr[2];
#pragma unroll
  for (int kk = 0; kk < 2; ++kk) a_addr[kk] = sbase + (wr * 64 + fr) * 128 + (((kk * 4 + fq) ^ aswz) * 16);
#pragma unroll
  for (int n = 0; n < 2; ++n) {
    const int t = wc * 2 + n;
    b_addr[n] = sbase + 4 * HT + (t >> 2) * 8192 + (fq * 8 + q4) * 128 + (((t & 3) ^ bswz) * 32) + p4 * 8;
  }
  bf16x8 At[4][2];
  s16x4 B0l[2][2], B0h[2][2], B1l[2][2], B1h[2][2];
#define LDA_(b, h)                                                                     \
  do {                                                                                 \
    lds_b128_issue<((b) * 2 + (h)) * HT + 0 * 2048>(At[0][0], a_addr[0]);              \
    lds_b128_issue<((b) * 2 + (h)) * HT + 1 * 2048>(At[1][0], a_addr[0]);              \
    lds_b128_issue<((b) * 2 + (h)) * HT + 2 * 2048>(At[2][0], a_addr[0]);              \
    lds_b128_issue<((b) * 2 + (h)) * HT + 3 * 2048>(At[3][0], a_addr[0]);              \
    lds_b128_issue<((b) * 2 + (h)) * HT + 0 * 2048>(At[0][1], a_addr[1]);              \
    lds_b128_issue<((b) * 2 + (h)) * HT + 1 * 2048>(At[1][1], a_addr[1]);              \
    lds_b128_issue<((b) * 2 + (h)) * HT + 2 * 2048>(At[2][1], a_addr[1]);              \
    lds_b128_issue<((b) * 2 + (h)) * HT + 3 * 2048>(At[3][1], a_addr[1]);              \
  } while (0)
#define LDB_(Bl, Bh, b, h)                                                             \
  do {                                                                                 \
    lds_tr_issue<((b) * 2 + (h)) * HT>(Bl[0][0], Bh[0][0], b_addr[0]);                 \
    lds_tr_issue<((b) * 2 + (h)) * HT>(Bl[1][0], Bh[1][0], b_addr[1]);                 \
    lds_tr_issue<((b) * 2 + (h)) * HT + 4096>(Bl[0][1], Bh[0][1], b_addr[0]);          \
    lds_tr_issue<((b) * 2 + (h)) * HT + 4096>(Bl[1][1], Bh[1][1], b_addr[1]);          \
  } while (0)
#define TIE_A()                                                                        \
  do {                                                                                 \
    _Pragma("unroll") for (int m = 0; m < 4; ++m) { opaque(At[m][0]); opaque(At[m][1]); } \
  } while (0)
#define TIE_B(Bl, Bh)                                                                  \
  do {                                                                                 \
    _Pragma("unroll") for (int n = 0; n < 2; ++n) { opaque(Bl[n][0]); opaque(Bl[n][1]); opaque(Bh[n][0]); opaque(Bh[n][1]); } \
  } while (0)
#define MMA_(ai, bj, Bl, Bh)                                                           \
  do {                                                                                 \
    __builtin_amdgcn_s_setprio(1);                                                     \
    _Pragma("unroll") for (int m = 0; m < 4; ++m)                                      \
    _Pragma("unroll") for (int n = 0; n < 2; ++n)                                      \
    _Pragma("unroll") for (int k = 0; k < 2; ++k)                                      \
      acc[ai][bj][m][n] = __builtin_amdgcn_mfma_f32_16x16x32_bf16(cat4(Bl[n][k], Bh[n][k]), At[m][k], acc[ai][bj][m][n], 0, 0, 0); \
    __builtin_amdgcn_s_setprio(0);                                                     \
  } while (0)
#define WAITV(n) asm volatile("s_waitcnt vmcnt(" #n ")" ::: "memory")
#define WAITL(n) asm volatile("s_waitcnt lgkmcnt(" #n ")" ::: "memory")
#define BAR_ __builtin_amdgcn_s_barrier()
#define SCHED_ __builtin_amdgcn_sched_barrier(0)
  STAGE_B(0, 0, 0); STAGE_A(0, 0, 0); STAGE_B(0, 1, 0); STAGE_A(0, 1, 0);
  if (wr == 1) BAR_;
  WAITV(4); BAR_;
  STAGE_B(1, 0, 1); STAGE_A(1, 0, 1); STAGE_B(1, 1, 1);
  WAITV(6); BAR_;
  for (int t = 0; t < nt - 2; t += 2) {
    LDB_(B0l, B0h, 0, 0); SCHED_; LDA_(0, 0); STAGE_A(1, 1, t + 1);
    WAITL(8); BAR_; WAITL(0); TIE_A(); TIE_B(B0l, B0h); MMA_(0, 0, B0l, B0h); BAR_; SCHED_;
    LDB_(B1l, B1h, 0, 1); STAGE_B(0, 0, t + 2);
    BAR_; WAITL(0); TIE_B(B1l, B1h); MMA_(0, 1, B1l, B1h); BAR_;
    LDA_(0, 1); STAGE_A(0, 0, t + 2);
    BAR_; WAITL(0); TIE_A(); MMA_(1, 0, B0l, B0h); BAR_; SCHED_;
    STAGE_B(0, 1, t + 2);
    WAITV(6); BAR_; MMA_(1, 1, B1l, B1h); BAR_;
    LDB_(B0l, B0h, 1, 0); SCHED_; LDA_(1, 0); STAGE_A(0, 1, t + 2);
    WAITL(8); BAR_; WAITL(0); TIE_A(); TIE_B(B0l, B0h); MMA_(0, 0, B0l, B0h); BAR_; SCHED_;
    LDB_(B1l, B1h, 1, 1); STAGE_B(1, 0, t + 3);
    BAR_; WAITL(0); TIE_B(B1l, B1h); MMA_(0, 1, B1l, B1h); BAR_;
    LDA_(1, 1); STAGE_A(1, 0, t + 3);
    BAR_; WAITL(0); TIE_A(); MMA_(1, 0, B0l, B0h); BAR_; SCHED_;
    STAGE_B(1, 1, t + 3);
    WAITV(6); BAR_; MMA_(1, 1, B1l, B1h); BAR_;
  }
  {
    LDB_(B0l, B0h, 0, 0); WAITV(0); LDA_(0, 0); STAGE_A(1, 1, nt - 1);
    BAR_; WAITL(0); TIE_A(); TIE_B(B0l, B0h); MMA_(0, 0, B0l, B0h); BAR_;
    LDB_(B1l, B1h, 0, 1); BAR_; WAITL(0); TIE_B(B1l, B1h); MMA_(0, 1, B1l, B1h); BAR_;
    LDA_(0, 1); WAITV(4); BAR_; WAITL(0); TIE_A(); MMA_(1, 0, B0l, B0h); MMA_(1, 1, B1l, B1h); BAR_;
  }
  {
    LDB_(B0l, B0h, 1, 0); LDA_(1, 0); WAITV(2); BAR_; WAITL(0); TIE_A(); TIE_B(B0l, B0h); MMA_(0, 0, B0l, B0h); BAR_;
    LDB_(B1l, B1h, 1, 1); WAITV(0); BAR_; WAITL(0); TIE_B(B1l, B1h); MMA_(0, 1, B1l, B1h); BAR_;
    LDA_(1, 1); BAR_; WAITL(0); TIE_A(); MMA_(1, 0, B0l, B0h); MMA_(1, 1, B1l, B1h); BAR_;
  }
  if (wr == 0) BAR_;
#undef SA_
#undef SB_
#undef STAGE_A
#undef STAGE_B
#undef LDA_
#undef LDB_
#undef TIE_A
#undef TIE_B
#undef MMA_
}

__global__ __launch_bounds__(512, 2) void gemm_gu8_kernel(const u16* __restrict__ xn, const u16* __restrict__ w1,
                                                          const u16* __restrict__ w3, u16* __restrict__ h) {
  __shared__ __attribute__((aligned(16))) char smem[8 * 16384];
  const int lane = threadIdx.x & 63, wid = threadIdx.x >> 6;
  const int L = (blockIdx.x & 7) * 28 + (blockIdx.x >> 3);
  const int colb = L >> 3, rowt = L & 7;
  const int row0 = rowt * 256, f0 = colb * 128;
  int krow;
  const int lc = bchunk_col(lane, wid, krow);
  const int ntl = lc >> 4;
  const u16* bp = ((ntl & 1) ? w3 : w1) + (size_t)krow * F_ + f0 + (ntl >> 1) * 16 + (lc & 15);
  f32x4 acc[2][2][4][2];
#pragma unroll
  for (int i = 0; i < 2; ++i)
#pragma unroll
    for (int j = 0; j < 2; ++j)
#pragma unroll
      for (int m = 0; m < 4; ++m)
#pragma unroll
        for (int n = 0; n < 2; ++n) acc[i][j][m][n] = (f32x4){0.f, 0.f, 0.f, 0.f};
  gemm256_8ph(xn + (size_t)row0 * D_, D_, bp, 32, F_, 16, smem, acc);
  int tz = threadIdx.x;
  asm volatile("" : "+v"(tz));
  const int l2 = tz & 63, w2i = tz >> 6, fr = l2 & 15, fq = l2 >> 4, wr = w2i >> 2, wc = w2i & 3;
#pragma unroll
  for (int ai = 0; ai < 2; ++ai)
#pragma unroll
    for (int bj = 0; bj < 2; ++bj) {
      const int col = f0 + bj * 64 + wc * 16 + fq * 4;
#pragma unroll
      for (int m = 0; m < 4; ++m) {
        const int row = row0 + ai * 128 + wr * 64 + m * 16 + fr;
        s16x4 o;
#pragma unroll
        for (int r = 0; r < 4; ++r) {
          const float g = rbf(acc[ai][bj][m][0][r]);
          const float u = rbf(acc[ai][bj][m][1][r]);
          o[r] = (short)f2bf(rbf(silu_f(g)) * u);
        }
        *reinterpret_cast<s16x4*>(h + (size_t)row * F_ + col) = o;
      }
    }
}

typedef GemmCfg<256, 256, 2, 4> CfgGU;
__global__ __launch_bounds__(512, 2) void gemm_gu_kernel(const u16* __restrict__ xn, const u16* __restrict__ w1,
                                                         const u16* __restrict__ w3, u16* __restrict__ h) {
  __shared__ __attribute__((aligned(16))) char smem[CfgGU::LDS];
  const int lane = threadIdx.x & 63, wid = threadIdx.x >> 6;
  const int L = (blockIdx.x & 7) * 28 + (blockIdx.x >> 3);
  const int colb = L >> 3, rowt = L & 7;
  const int row0 = rowt * 256, f0 = colb * 128;
  int krow;
  const int lc = bchunk_col(lane, wid, krow);
  const int ntl = lc >> 4;
  const u16* bp = ((ntl & 1) ? w3 : w1) + (size_t)krow * F_ + f0 + (ntl >> 1) * 16 + (lc & 15);
  f32x4 acc[CfgGU::MT][CfgGU::NT];
#pragma unroll
  for (int m = 0; m < CfgGU::MT; ++m)
#pragma unroll
    for (int n = 0; n < CfgGU::NT; ++n) acc[m][n] = (f32x4){0.f, 0.f, 0.f, 0.f};
  gemm_mainloop<CfgGU>(xn + (size_t)row0 * D_, D_, bp, 32, F_, 16, smem, acc);
  int tz = threadIdx.x;
  asm volatile("" : "+v"(tz));
  const int l2 = tz & 63, w2i = tz >> 6, fr = l2 & 15, fq = l2 >> 4, wm = w2i >> 2, wn = w2i & 3;
#pragma unroll
  for (int n = 0; n < CfgGU::NT; n += 2) {
    const int col = f0 + wn * 32 + (n >> 1) * 16 + fq * 4;
#pragma unroll
    for (int m = 0; m < CfgGU::MT; ++m) {
      const int row = row0 + wm * 128 + m * 16 + fr;
      s16x4 o;
#pragma unroll
      for (int r = 0; r < 4; ++r) {
        const float g = rbf(acc[m][n][r]);
        const float u = rbf(acc[m][n + 1][r]);
        o[r] = (short)f2bf(rbf(silu_f(g)) * u);
      }
      *reinterpret_cast<s16x4*>(h + (size_t)row * F_ + col) = o;
    }
  }
}

typedef GemmCfg<128, 192, 2, 4> CfgQKV;
__global__ __launch_bounds__(512, 2) void gemm_qkv_kernel(const u16* __restrict__ xn, const u16* __restrict__ wqkv,
                                                          u16* __restrict__ qkv) {
  __shared__ __attribute__((aligned(16))) char smem[CfgQKV::LDS];
  const int lane = threadIdx.x & 63, wid = threadIdx.x >> 6;
  const int L = (blockIdx.x & 7) * 32 + (blockIdx.x >> 3);
  const int colb = L >> 4, rowt = L & 15;
  const int row0 = rowt * 128, col0 = colb * 192;
  int krow;
  const int lc = bchunk_col(lane, wid, krow);
  const u16* bp = wqkv + (size_t)krow * 3072 + col0 + lc;
  f32x4 acc[CfgQKV::MT][CfgQKV::NT];
#pragma unroll
  for (int m = 0; m < CfgQKV::MT; ++m)
#pragma unroll
    for (int n = 0; n < CfgQKV::NT; ++n) acc[m][n] = (f32x4){0.f, 0.f, 0.f, 0.f};
  gemm_mainloop<CfgQKV>(xn + (size_t)row0 * D_, D_, bp, 64, 3072, 16, smem, acc);
  int tz = threadIdx.x;
  asm volatile("" : "+v"(tz));
  const int l2 = tz & 63, w2i = tz >> 6, fr = l2 & 15, fq = l2 >> 4, wm = w2i >> 2, wn = w2i & 3;
#pragma unroll
  for (int n = 0; n < CfgQKV::NT; ++n) {
    const int col = col0 + wn * 48 + n * 16 + fq * 4;
#pragma unroll
    for (int m = 0; m < CfgQKV::MT; ++m) {
      const int row = row0 + wm * 64 + m * 16 + fr;
      s16x4 o;
#pragma unroll
      for (int r = 0; r < 4; ++r) o[r] = (short)f2bf(acc[m][n][r]);
      *reinterpret_cast<s16x4*>(qkv + (size_t)row * 3072 + col) = o;
    }
  }
}

__global__ __launch_bounds__(256) void attn_kernel(const u16* __restrict__ qkv, u16* __restrict__ o) {
  const int idx = blockIdx.x * 256 + threadIdx.x;
  const int c = idx & 3, hh = (idx >> 2) & 31, s = idx >> 7;
  const u16* base = qkv + hh * 32 + c * 8;
  bf16x8 qv = *reinterpret_cast<const bf16x8*>(base + (size_t)s * 3072);
  float qf[8];
#pragma unroll
  for (int i = 0; i < 8; ++i) qf[i] = bf2f((u16)qv[i]);
  float lg[8];
  const float scale = 0.17677669529663687f;
#pragma unroll
  for (int j = 0; j < 8; ++j) {
    const int pos = s - 2 * j;
    float d = 0.f;
    if (pos >= 0) {
      bf16x8 kv = *reinterpret_cast<const bf16x8*>(base + (size_t)pos * 3072 + 1024);
#pragma unroll
      for (int i = 0; i < 8; ++i) d += qf[i] * bf2f((u16)kv[i]);
    }
    d += __shfl_xor(d, 1);
    d += __shfl_xor(d, 2);
    lg[j] = (pos >= 0) ? rbf(rbf(d) * scale) : -1e9f;
  }
  float mx = lg[0];
#pragma unroll
  for (int j = 1; j < 8; ++j) mx = fmaxf(mx, lg[j]);
  float e[8], sum = 0.f;
#pragma unroll
  for (int j = 0; j < 8; ++j) { e[j] = __expf(lg[j] - mx); sum += e[j]; }
  const float inv = 1.0f / sum;
  float of[8];
#pragma unroll
  for (int i = 0; i < 8; ++i) of[i] = 0.f;
#pragma unroll
  for (int j = 0; j < 8; ++j) {
    const int pos = s - 2 * j;
    if (pos >= 0) {
      const float a = rbf(e[j] * inv);
      bf16x8 vv = *reinterpret_cast<const bf16x8*>(base + (size_t)pos * 3072 + 2048);
#pragma unroll
      for (int i = 0; i < 8; ++i) of[i] += a * bf2f((u16)vv[i]);
    }
  }
  bf16x8 ov;
#pragma unroll
  for (int i = 0; i < 8; ++i) ov[i] = (short)f2bf(of[i]);
  *reinterpret_cast<bf16x8*>(o + (size_t)s * D_ + hh * 32 + c * 8) = ov;
}

constexpr int G2_ASZ = 128 * 128;
constexpr int G2_BSZ = 2 * 8192;
constexpr int G2_STAGE = G2_ASZ + G2_BSZ;

__device__ __forceinline__ void gemm2_loop(const u16* __restrict__ A, int lda, const u16* __restrict__ B,
                                           int row0, int col0, int k0, int nkt, char* smem, f32x4 (&acc)[4][4]) {
  const int tid = threadIdx.x, lane = tid & 63, wid = tid >> 6;
  const int wr = wid >> 1, wc = wid & 1;
  const int fr = lane & 15, fq = lane >> 4;
  const u16* asrc[4];
#pragma unroll
  for (int j = 0; j < 4; ++j) {
    const int r = wid * 32 + j * 8 + (lane >> 3);
    const int c = (lane & 7) ^ ((r >> 1) & 7);
    asrc[j] = A + (size_t)(row0 + r) * lda + k0 + c * 8;
  }
  const u16* bsrc[2][2];
#pragma unroll
  for (int j = 0; j < 2; ++j) {
    const int k = wid * 16 + j * 8 + (lane >> 3);
    const int p16 = lane & 7;
    const int g = ((k >> 1) & 1) | (((k >> 3) & 1) << 1);
    const int c = (p16 >> 1) ^ g, half = p16 & 1;
#pragma unroll
    for (int s = 0; s < 2; ++s) bsrc[s][j] = B + (size_t)(k0 + k) * D_ + col0 + s * 64 + c * 16 + half * 8;
  }
  auto stage = [&](int kt, int buf) {
    char* sa = smem + buf * G2_STAGE;
    char* sb = sa + G2_ASZ;
#pragma unroll
    for (int j = 0; j < 4; ++j) glds16(asrc[j] + kt * 64, sa + (wid * 32 + j * 8) * 128 + lane * 16);
#pragma unroll
    for (int s = 0; s < 2; ++s)
#pragma unroll
      for (int j = 0; j < 2; ++j)
        glds16(bsrc[s][j] + (size_t)kt * 64 * D_, sb + s * 8192 + (wid * 16 + j * 8) * 128 + lane * 16);
  };
  const int aswz = (fr >> 1) & 7;
  const int q4 = (lane & 15) >> 2, p4 = lane & 3;
  const int bswz = (q4 >> 1) | ((fq & 1) << 1);

  const unsigned sbase = lds_addr(smem);
  unsigned a_addr[2], b_addr[4];
#pragma unroll
  for (int kk = 0; kk < 2; ++kk) a_addr[kk] = sbase + (wr * 64 + fr) * 128 + (((kk * 4 + fq) ^ aswz) * 16);
#pragma unroll
  for (int n = 0; n < 4; ++n) {
    const int nt = wc * 4 + n;
    b_addr[n] = sbase + G2_ASZ + (nt >> 2) * 8192 + (fq * 8 + q4) * 128 + (((nt & 3) ^ bswz) * 32) + p4 * 8;
  }
  stage(0, 0);
  asm volatile("s_waitcnt vmcnt(0)" ::: "memory");
  __syncthreads();
  for (int kt = 0; kt < nkt; ++kt) {
    const int buf = kt & 1;
    if (kt + 1 < nkt) stage(kt + 1, buf ^ 1);
    const unsigned so = buf * G2_STAGE;
#pragma unroll
    for (int kk = 0; kk < 2; ++kk) {
      s16x4 bl[4], bh[4];
      bf16x8 a[4];
#pragma unroll
      for (int n = 0; n < 4; ++n) {
        if (kk == 0) lds_tr_issue<0>(bl[n], bh[n], b_addr[n] + so);
        else lds_tr_issue<4096>(bl[n], bh[n], b_addr[n] + so);
      }
      const unsigned aa = a_addr[kk] + so;
      lds_b128_issue<0 * 2048>(a[0], aa);
      lds_b128_issue<1 * 2048>(a[1], aa);
      lds_b128_issue<2 * 2048>(a[2], aa);
      lds_b128_issue<3 * 2048>(a[3], aa);
      asm volatile("s_waitcnt lgkmcnt(0)"
                   : "+v"(bl[0]), "+v"(bh[0]), "+v"(bl[1]), "+v"(bh[1]), "+v"(bl[2]), "+v"(bh[2]), "+v"(bl[3]),
                     "+v"(bh[3]), "+v"(a[0]), "+v"(a[1]), "+v"(a[2]), "+v"(a[3])::"memory");
#pragma unroll
      for (int m = 0; m < 4; ++m)
#pragma unroll
        for (int n = 0; n < 4; ++n)
          acc[m][n] = __builtin_amdgcn_mfma_f32_16x16x32_bf16(cat4(bl[n], bh[n]), a[m], acc[m][n], 0, 0, 0);
    }
    asm volatile("s_waitcnt vmcnt(0)" ::: "memory");
    __syncthreads();
  }
}

__global__ __launch_bounds__(256, 1) void gemm2_kernel(const u16* __restrict__ o, const u16* __restrict__ wo,
                                                       const u16* __restrict__ h, const u16* __restrict__ w2,
                                                       u16* __restrict__ sbuf, float* __restrict__ g0,
                                                       float* __restrict__ g1) {
  __shared__ __attribute__((aligned(16))) char smem[2 * G2_STAGE];
  const int tid = threadIdx.x, lane = tid & 63, wid = tid >> 6;
  const int wr = wid >> 1, wc = wid & 1;
  const int fr = lane & 15, fq = lane >> 4;
  const int xcd = blockIdx.x & 7, idx = blockIdx.x >> 3;
  const int split = idx & 1;
  const int t = idx >> 1;
  const int tn = (xcd & 1) * 4 + (t & 3);
  const int tm = (xcd >> 1) * 4 + (t >> 2);
  const int row0 = tm * 128, col0 = tn * 128;
  f32x4 acc[4][4];
#pragma unroll
  for (int m = 0; m < 4; ++m)
#pragma unroll
    for (int n = 0; n < 4; ++n) acc[m][n] = (f32x4){0.f, 0.f, 0.f, 0.f};
  if (split == 0) {
    gemm2_loop(o, D_, wo, row0, col0, 0, 16, smem, acc);
#pragma unroll
    for (int m = 0; m < 4; ++m)
#pragma unroll
      for (int n = 0; n < 4; ++n) {
        const int row = row0 + wr * 64 + m * 16 + fr, col = col0 + wc * 64 + n * 16 + fq * 4;
        s16x4 ov;
#pragma unroll
        for (int r = 0; r < 4; ++r) ov[r] = (short)f2bf(acc[m][n][r]);
        *reinterpret_cast<s16x4*>(sbuf + (size_t)row * D_ + col) = ov;
        acc[m][n] = (f32x4){0.f, 0.f, 0.f, 0.f};
      }
    gemm2_loop(h, F_, w2, row0, col0, 0, 20, smem, acc);
  } else {
    gemm2_loop(h, F_, w2, row0, col0, 1280, 36, smem, acc);
  }
  float* gd = split == 0 ? g0 : g1;
#pragma unroll
  for (int m = 0; m < 4; ++m)
#pragma unroll
    for (int n = 0; n < 4; ++n) {
      const int row = row0 + wr * 64 + m * 16 + fr, col = col0 + wc * 64 + n * 16 + fq * 4;
      *reinterpret_cast<f32x4*>(gd + (size_t)row * D_ + col) = acc[m][n];
    }
}

__global__ __launch_bounds__(256) void final_kernel(const u16* __restrict__ x, const u16* __restrict__ sbuf,
                                                    const float* __restrict__ g0, const float* __restrict__ g1,
                                                    u16* __restrict__ out) {
  const size_t i = ((size_t)blockIdx.x * 256 + threadIdx.x) * 8;
  bf16x8 xv = *reinterpret_cast<const bf16x8*>(x + i);
  bf16x8 sv = *reinterpret_cast<const bf16x8*>(sbuf + i);
  f32x4 a0 = *reinterpret_cast<const f32x4*>(g0 + i), a1 = *reinterpret_cast<const f32x4*>(g0 + i + 4);
  f32x4 b0 = *reinterpret_cast<const f32x4*>(g1 + i), b1 = *reinterpret_cast<const f32x4*>(g1 + i + 4);
  bf16x8 ov;
#pragma unroll
  for (int k = 0; k < 8; ++k) {
    const float g = rbf(k < 4 ? a0[k] + b0[k] : a1[k - 4] + b1[k - 4]);
    const float sg = rbf(silu_f(g));
    const float p = rbf(bf2f((u16)sv[k]) * sg);
    ov[k] = (short)f2bf(bf2f((u16)xv[k]) + p);
  }
  *reinterpret_cast<bf16x8*>(out + i) = ov;
}

extern "C" void kernel_launch(void* const* d_in, const int* in_sizes, int n_in, void* d_out, int out_size,
                              void* d_ws, size_t ws_size, hipStream_t stream) {
  const u16* x = (const u16*)d_in[0];
  const u16* wqkv = (const u16*)d_in[1];
  const u16* wo = (const u16*)d_in[2];
  const u16* w1 = (const u16*)d_in[4];
  const u16* w3 = (const u16*)d_in[5];
  const u16* w2 = (const u16*)d_in[6];
  u16* out = (u16*)d_out;
  char* ws = (char*)d_ws;
  u16* xn = (u16*)ws;
  u16* qkv = (u16*)(ws + (4u << 20));
  u16* h = (u16*)(ws + (16u << 20));
  u16* o = (u16*)(ws + (32u << 20));
  u16* sbuf = (u16*)(ws + (36u << 20));
  float* g0 = (float*)(ws + (40u << 20));
  float* g1 = (float*)(ws + (48u << 20));

  ln_kernel<<<S_ / 4, 256, 0, stream>>>(x, xn);
  gemm_gu8_kernel<<<224, 512, 0, stream>>>(xn, w1, w3, h);
  gemm_qkv_kernel<<<256, 512, 0, stream>>>(xn, wqkv, qkv);
  attn_kernel<<<S_ * NH * 4 / 256, 256, 0, stream>>>(qkv, o);
  gemm2_kernel<<<256, 256, 0, stream>>>(o, wo, h, w2, sbuf, g0, g1);
  final_kernel<<<S_ * D_ / 8 / 256, 256, 0, stream>>>(x, sbuf, g0, g1, out);
}
```

```cpp
#include <hip/hip_runtime.h>
#include <hip/hip_bf16.h>
#include <stdint.h>
#include <type_traits>

typedef __attribute__((ext_vector_type(8))) short bf16x8;
typedef __attribute__((ext_vector_type(4))) short s16x4;
typedef __attribute__((ext_vector_type(4))) float f32x4;
typedef unsigned short u16;
typedef __attribute__((address_space(3))) s16x4* lds_s16x4_p;

constexpr int S_ = 2048, D_ = 1024, F_ = 3584, NH = 32, DH = 32;

__device__ __forceinline__ u16 f2bf(float f) {
  uint32_t u = __float_as_uint(f);
  u += 0x7fffu + ((u >> 16) & 1u);
  return (u16)(u >> 16);
}
__device__ __forceinline__ float bf2f(u16 h) { return __uint_as_float(((uint32_t)h) << 16); }
__device__ __forceinline__ float rbf(float f) { return bf2f(f2bf(f)); }
__device__ __forceinline__ float silu_f(float v) { return v / (1.0f + __expf(-v)); }

__device__ __forceinline__ void glds16(const void* g, void* l) {
  __builtin_amdgcn_global_load_lds((const __attribute__((address_space(1))) unsigned*)g,
                                   (__attribute__((address_space(3))) unsigned*)l, 16, 0, 0);
}
__device__ __forceinline__ bf16x8 lds_read128(const char* p) {
  return *reinterpret_cast<const bf16x8*>(p);
}
template <int OFF>
__device__ __forceinline__ void lds_tr_issue(s16x4& a, s16x4& b, unsigned addr) {
  asm volatile("ds_read_b64_tr_b16 %0, %2 offset:%3\n\tds_read_b64_tr_b16 %1, %2 offset:%4"
               : "=&v"(a), "=&v"(b)
               : "v"(addr), "i"(OFF), "i"(OFF + 512)
               : "memory");
}
template <int OFF>
__device__ __forceinline__ void lds_b128_issue(bf16x8& a, unsigned addr) {
  asm volatile("ds_read_b128 %0, %1 offset:%2" : "=&v"(a) : "v"(addr), "i"(OFF) : "memory");
}
template <int I, int MT, int OFF>
__device__ __forceinline__ void lds_a_issue_rec(bf16x8 (&a)[MT], unsigned addr) {
  if constexpr (I < MT) {
    lds_b128_issue<OFF + I * 2048>(a[I], addr);
    lds_a_issue_rec<I + 1, MT, OFF>(a, addr);
  }
}
template <int MT, int OFF>
__device__ __forceinline__ void lds_a_issue(bf16x8 (&a)[MT], unsigned addr) { lds_a_issue_rec<0, MT, OFF>(a, addr); }
__device__ __forceinline__ void opaque(s16x4& v) { asm volatile("" : "+v"(v)); }
__device__ __forceinline__ void opaque(bf16x8& v) { asm volatile("" : "+v"(v)); }
template <int NT, int MT>
__device__ __forceinline__ void lds_wait_all(s16x4 (&bl)[NT], s16x4 (&bh)[NT], bf16x8 (&a)[MT]) {
  asm volatile("s_waitcnt lgkmcnt(0)" ::: "memory");
#pragma unroll
  for (int n = 0; n < NT; ++n) { opaque(bl[n]); opaque(bh[n]); }
#pragma unroll
  for (int m = 0; m < MT; ++m) opaque(a[m]);
}
__device__ __forceinline__ bf16x8 cat4(s16x4 a, s16x4 b) {
  return __builtin_shufflevector(a, b, 0, 1, 2, 3, 4, 5, 6, 7);
}
__device__ __forceinline__ unsigned lds_addr(const void* p) {
  return (unsigned)(uintptr_t)(const __attribute__((address_space(3))) char*)p;
}

__global__ __launch_bounds__(256) void ln_kernel(const u16* __restrict__ x, u16* __restrict__ xn) {
  const int lane = threadIdx.x & 63;
  const int row = blockIdx.x * 4 + (threadIdx.x >> 6);
  const u16* xr = x + (size_t)row * D_;
  bf16x8 v0 = *reinterpret_cast<const bf16x8*>(xr + lane * 8);
  bf16x8 v1 = *reinterpret_cast<const bf16x8*>(xr + 512 + lane * 8);
  float f[16];
#pragma unroll
  for (int i = 0; i < 8; ++i) { f[i] = bf2f((u16)v0[i]); f[8 + i] = bf2f((u16)v1[i]); }
  float s = 0.f;
#pragma unroll
  for (int i = 0; i < 16; ++i) s += f[i];
#pragma unroll
  for (int o = 32; o >= 1; o >>= 1) s += __shfl_xor(s, o);
  const float mean = s * (1.0f / D_);
  float q = 0.f;
#pragma unroll
  for (int i = 0; i < 16; ++i) { float d = f[i] - mean; q += d * d; }
#pragma unroll
  for (int o = 32; o >= 1; o >>= 1) q += __shfl_xor(q, o);
  const float var = q * (1.0f / D_);
  const float mb = rbf(mean);
  const float vb = rbf(var);
  const float den = rbf(sqrtf(rbf(vb + 1e-5f)));
  bf16x8 o0, o1;
#pragma unroll
  for (int i = 0; i < 8; ++i) {
    o0[i] = (short)f2bf(rbf(f[i] - mb) / den);
    o1[i] = (short)f2bf(rbf(f[8 + i] - mb) / den);
  }
  u16* orow = xn + (size_t)row * D_;
  *reinterpret_cast<bf16x8*>(orow + lane * 8) = o0;
  *reinterpret_cast<bf16x8*>(orow + 512 + lane * 8) = o1;
}

template <int BM, int BN, int WAVES_M, int WAVES_N>
struct GemmCfg {
  static constexpr int NWN = WAVES_N;
  static constexpr int WM = BM / WAVES_M, WN = BN / WAVES_N;
  static constexpr int MT = WM / 16, NT = WN / 16;
  static constexpr int ASZ = BM * 128, BSZ = (BN / 64) * 8192;
  static constexpr int LDS = 2 * (ASZ + BSZ);
  static constexpr int AI = BM / 64;
  static constexpr int BI = BN / 64;
};

template <class C>
__device__ __forceinline__ void gemm_mainloop(const u16* __restrict__ aptr, int lda, const u16* __restrict__ bptr,
                                              int bsub, int ldb, int nkt, char* smem,
                                              f32x4 (&acc)[C::MT][C::NT]) {
  const int tid = threadIdx.x, lane = tid & 63, wid = tid >> 6;
  const int wu = __builtin_amdgcn_readfirstlane(wid);
  const int wm = wu / C::NWN, wn = wu % C::NWN;
  const int fr = lane & 15, fq = lane >> 4;
  unsigned aoff[C::AI];
#pragma unroll
  for (int j = 0; j < C::AI; ++j) {
    const int r = wu * (C::AI * 8) + j * 8 + (lane >> 3);
    const int c = (lane & 7) ^ ((r >> 1) & 7);
    aoff[j] = (unsigned)(r * lda + c * 8) * 2u;
  }
  const char* abase = (const char*)aptr;
  auto stage = [&](int kt, int buf) {
    char* sa = smem + buf * C::ASZ + wu * (C::AI * 1024);
    char* sb = smem + 2 * C::ASZ + buf * C::BSZ + wu * 1024;
#pragma unroll
    for (int j = 0; j < C::AI; ++j) glds16(abase + kt * 128 + aoff[j], sa + j * 1024);
    const u16* bp = bptr + (size_t)kt * 64 * ldb;
#pragma unroll
    for (int s = 0; s < C::BI; ++s) glds16(bp + s * bsub, sb + s * 8192);
  };
  const int aswz = (fr >> 1) & 7;
  const int q4 = (lane & 15) >> 2, p4 = lane & 3;
  const int bswz = (q4 >> 1) | ((fq & 1) << 1);
  const unsigned sbase = lds_addr(smem);
  unsigned a_addr[2], b_addr[C::NT];
#pragma unroll
  for (int kk = 0; kk < 2; ++kk) a_addr[kk] = sbase + (wm * C::WM + fr) * 128 + (((kk * 4 + fq) ^ aswz) * 16);
#pragma unroll
  for (int n = 0; n < C::NT; ++n) {
    const int nt = wn * C::NT + n;
    b_addr[n] = sbase + 2 * C::ASZ + (nt >> 2) * 8192 + (fq * 8 + q4) * 128 + (((nt & 3) ^ bswz) * 32) + p4 * 8;
  }
  auto compute = [&](auto bufc) {
    constexpr int BUF = decltype(bufc)::value;
#pragma unroll
    for (int kk = 0; kk < 2; ++kk) {
      s16x4 bl[C::NT], bh[C::NT];
      bf16x8 a[C::MT];
#pragma unroll
      for (int n = 0; n < C::NT; ++n) {
        if (kk == 0) lds_tr_issue<BUF * C::BSZ>(bl[n], bh[n], b_addr[n]);
        else lds_tr_issue<BUF * C::BSZ + 4096>(bl[n], bh[n], b_addr[n]);
      }
      lds_a_issue<C::MT, BUF * C::ASZ>(a, a_addr[kk]);
      lds_wait_all<C::NT, C::MT>(bl, bh, a);
#pragma unroll
      for (int m = 0; m < C::MT; ++m)
#pragma unroll
        for (int n = 0; n < C::NT; ++n)
          acc[m][n] = __builtin_amdgcn_mfma_f32_16x16x32_bf16(cat4(bl[n], bh[n]), a[m], acc[m][n], 0, 0, 0);
    }
  };
  stage(0, 0);
  asm volatile("s_waitcnt vmcnt(0)" ::: "memory");
  __builtin_amdgcn_s_barrier();
  for (int kt = 0; kt < nkt; kt += 2) {
    stage(kt + 1, 1);
    compute(std::integral_constant<int, 0>{});
    asm volatile("s_waitcnt vmcnt(0)" ::: "memory");
    __builtin_amdgcn_s_barrier();
    if (kt + 2 < nkt) stage(kt + 2, 0);
    compute(std::integral_constant<int, 1>{});
    asm volatile("s_waitcnt vmcnt(0)" ::: "memory");
    __builtin_amdgcn_s_barrier();
  }
}

#define BN_SUBS(C) (C::BSZ / 8192)
template <int N>
__device__ __forceinline__ void wait_vm() { asm volatile("s_waitcnt vmcnt(%0)" ::"i"(N) : "memory"); }
template <int N>
__device__ __forceinline__ void wait_lgkm() { asm volatile("s_waitcnt lgkmcnt(%0)" ::"i"(N) : "memory"); }
template <int BM, int BN, int WAVES_M, int WAVES_N, int NST_>
struct RingCfg {
  static constexpr int NW = WAVES_M * WAVES_N, NWN = WAVES_N, NST = NST_;
  static constexpr int WM = BM / WAVES_M, WN = BN / WAVES_N;
  static constexpr int MT = WM / 16, NT = WN / 16;
  static constexpr int ASZ = BM * 128, BSZ = (BN / 64) * 8192, STG = ASZ + BSZ;
  static constexpr int LDS = NST * STG;
  static constexpr int AI = BM / 8 / NW;
  static constexpr int BR = 64 / NW / 8;
  static constexpr int BI = (BN / 64) * BR;
  static constexpr int G = AI + BI;
};
template <class C>
__device__ __forceinline__ void gemm_ring(const u16* __restrict__ aptr, int lda, const u16* __restrict__ bptr,
                                          int bsub, int ldb, int nkt, char* smem, f32x4 (&acc)[C::MT][C::NT],
                                          int bodd = 0) {
  const int tid = threadIdx.x, lane = tid & 63;
  const int wu = __builtin_amdgcn_readfirstlane(tid >> 6);
  const int wm = wu / C::NWN, wn = wu % C::NWN;
  const int fr = lane & 15, fq = lane >> 4;
  unsigned aoff[C::AI];
#pragma unroll
  for (int j = 0; j < C::AI; ++j) {
    const int r = wu * (C::AI * 8) + j * 8 + (lane >> 3);
    const int c = (lane & 7) ^ ((r >> 1) & 7);
    aoff[j] = (unsigned)(r * lda + c * 8) * 2u;
  }
  const char* abase = (const char*)aptr;
  auto stage = [&](int kt, int buf) {
    char* sa = smem + buf * C::STG + wu * (C::AI * 1024);
    char* sb = smem + buf * C::STG + C::ASZ + wu * (C::BR * 1024);
#pragma unroll
    for (int j = 0; j < C::AI; ++j) glds16(abase + kt * 128 + aoff[j], sa + j * 1024);
    const u16* bp = bptr + (size_t)kt * 64 * ldb;
#pragma unroll
    for (int s = 0; s < BN_SUBS(C); ++s)
#pragma unroll
      for (int j = 0; j < C::BR; ++j) glds16(bp + s * bsub + (size_t)j * 8 * ldb + ((j & 1) ? bodd : 0), sb + s * 8192 + j * 1024);
  };
  const int aswz = (fr >> 1) & 7;
  const int q4 = (lane & 15) >> 2, p4 = lane & 3;
  const int bswz = (q4 >> 1) | ((fq & 1) << 1);
  const unsigned sbase = lds_addr(smem);
  unsigned a_addr[2], b_addr[C::NT];
#pragma unroll
  for (int kk = 0; kk < 2; ++kk) a_addr[kk] = sbase + (wm * C::WM + fr) * 128 + (((kk * 4 + fq) ^ aswz) * 16);
#pragma unroll
  for (int n = 0; n < C::NT; ++n) {
    const int nt = wn * C::NT + n;
    b_addr[n] = sbase + C::ASZ + (nt >> 2) * 8192 + (fq * 8 + q4) * 128 + (((nt & 3) ^ bswz) * 32) + p4 * 8;
  }
#pragma unroll
  for (int i = 0; i < C::NST - 1; ++i)
    if (i < nkt) stage(i, i);
  int cur = 0, nxt = C::NST - 1;
  for (int kt = 0; kt < nkt; ++kt) {
    if (kt + C::NST - 2 < nkt) wait_vm<(C::NST - 2) * C::G>();
    else wait_vm<0>();
    __builtin_amdgcn_s_barrier();
    if (kt + C::NST - 1 < nkt) stage(kt + C::NST - 1, nxt);
    const unsigned bo = cur * C::STG;
    s16x4 bl[2][C::NT], bh[2][C::NT];
    bf16x8 a[2][C::MT];
#pragma unroll
    for (int n = 0; n < C::NT; ++n) lds_tr_issue<0>(bl[0][n], bh[0][n], b_addr[n] + bo);
    lds_a_issue<C::MT, 0>(a[0], a_addr[0] + bo);
#pragma unroll
    for (int n = 0; n < C::NT; ++n) lds_tr_issue<4096>(bl[1][n], bh[1][n], b_addr[n] + bo);
    lds_a_issue<C::MT, 0>(a[1], a_addr[1] + bo);
    wait_lgkm<2 * C::NT + C::MT>();
#pragma unroll
    for (int n = 0; n < C::NT; ++n) { opaque(bl[0][n]); opaque(bh[0][n]); }
#pragma unroll
    for (int m = 0; m < C::MT; ++m) opaque(a[0][m]);
#pragma unroll
    for (int m = 0; m < C::MT; ++m)
#pragma unroll
      for (int n = 0; n < C::NT; ++n)
        acc[m][n] = __builtin_amdgcn_mfma_f32_16x16x32_bf16(cat4(bl[0][n], bh[0][n]), a[0][m], acc[m][n], 0, 0, 0);
    wait_lgkm<0>();
#pragma unroll
    for (int n = 0; n < C::NT; ++n) { opaque(bl[1][n]); opaque(bh[1][n]); }
#pragma unroll
    for (int m = 0; m < C::MT; ++m) opaque(a[1][m]);
#pragma unroll
    for (int m = 0; m < C::MT; ++m)
#pragma unroll
      for (int n = 0; n < C::NT; ++n)
        acc[m][n] = __builtin_amdgcn_mfma_f32_16x16x32_bf16(cat4(bl[1][n], bh[1][n]), a[1][m], acc[m][n], 0, 0, 0);
    cur = (cur + 1 == C::NST) ? 0 : cur + 1;
    nxt = (nxt + 1 == C::NST) ? 0 : nxt + 1;
  }
  __builtin_amdgcn_s_barrier();
}

__device__ __forceinline__ int bchunk_col(int lane, int wid, int& krow) {
  krow = wid * 8 + (lane >> 3);
  const int p16 = lane & 7;
  const int g = ((krow >> 1) & 1) | (((krow >> 3) & 1) << 1);
  const int c = (p16 >> 1) ^ g, half = p16 & 1;
  return c * 16 + half * 8;
}

__device__ __forceinline__ void gemm256_8ph(const u16* __restrict__ aptr, int lda, const u16* __restrict__ bptr,
                                            int bsub, int ldb, int nt, char* smem, f32x4 (&acc)[2][2][4][2]) {
  const int tid = threadIdx.x, lane = tid & 63;
  const int wu = __builtin_amdgcn_readfirstlane(tid >> 6);
  const int wr = wu >> 2, wc = wu & 3;
  const int fr = lane & 15, fq = lane >> 4;
  constexpr int HT = 16384;
  unsigned aoff[2];
#pragma unroll
  for (int j = 0; j < 2; ++j) {
    const int r = wu * 16 + j * 8 + (lane >> 3);
    const int c = (lane & 7) ^ ((r >> 1) & 7);
    aoff[j] = (unsigned)(r * lda + c * 8) * 2u;
  }
  const char* abase = (const char*)aptr;
  const size_t ahalf = (size_t)128 * lda * 2;
#define SA_(b, h) (smem + ((b) * 2 + (h)) * HT)
#define SB_(b, h) (smem + 4 * HT + ((b) * 2 + (h)) * HT)
#define STAGE_A(b, h, kt)                                                              \
  do {                                                                                 \
    const char* _g = abase + (h) * ahalf + (size_t)(kt) * 128;                         \
    glds16(_g + aoff[0], SA_(b, h) + wu * 2048);                                       \
    glds16(_g + aoff[1], SA_(b, h) + wu * 2048 + 1024);                                \
  } while (0)
#define STAGE_B(b, h, kt)                                                              \
  do {                                                                                 \
    const u16* _g = bptr + (size_t)(kt) * 64 * ldb + (h) * 2 * bsub;                   \
    glds16(_g, SB_(b, h) + wu * 1024);                                                 \
    glds16(_g + bsub, SB_(b, h) + 8192 + wu * 1024);                                   \
  } while (0)
  const int aswz = (fr >> 1) & 7;
  const int q4 = (lane & 15) >> 2, p4 = lane & 3;
  const int bswz = (q4 >> 1) | ((fq & 1) << 1);
  const unsigned sbase = lds_addr(smem);
  unsigned a_addr[2], b_addr[2];
#pragma unroll
  for (int kk = 0; kk < 2; ++kk) a_addr[kk] = sbase + (wr * 64 + fr) * 128 + (((kk * 4 + fq) ^ aswz) * 16);
#pragma unroll
  for (int n = 0; n < 2; ++n) {
    const int t = wc * 2 + n;
    b_addr[n] = sbase + 4 * HT + (t >> 2) * 8192 + (fq * 8 + q4) * 128 + (((t & 3) ^ bswz) * 32) + p4 * 8;
  }
  bf16x8 At[4][2];
  s16x4 B0l[2][2], B0h[2][2], B1l[2][2], B1h[2][2];
#define LDA_(b, h)                                                                     \
  do {                                                                                 \
    lds_b128_issue<((b) * 2 + (h)) * HT + 0 * 2048>(At[0][0], a_addr[0]);              \
    lds_b128_issue<((b) * 2 + (h)) * HT + 1 * 2048>(At[1][0], a_addr[0]);              \
    lds_b128_issue<((b) * 2 + (h)) * HT + 2 * 2048>(At[2][0], a_addr[0]);              \
    lds_b128_issue<((b) * 2 + (h)) * HT + 3 * 2048>(At[3][0], a_addr[0]);              \
    lds_b128_issue<((b) * 2 + (h)) * HT + 0 * 2048>(At[0][1], a_addr[1]);              \
    lds_b128_issue<((b) * 2 + (h)) * HT + 1 * 2048>(At[1][1], a_addr[1]);              \
    lds_b128_issue<((b) * 2 + (h)) * HT + 2 * 2048>(At[2][1], a_addr[1]);              \
    lds_b128_issue<((b) * 2 + (h)) * HT + 3 * 2048>(At[3][1], a_addr[1]);              \
  } while (0)
#define LDB_(Bl, Bh, b, h)                                                             \
  do {                                                                                 \
    lds_tr_issue<((b) * 2 + (h)) * HT>(Bl[0][0], Bh[0][0], b_addr[0]);                 \
    lds_tr_issue<((b) * 2 + (h)) * HT>(Bl[1][0], Bh[1][0], b_addr[1]);                 \
    lds_tr_issue<((b) * 2 + (h)) * HT + 4096>(Bl[0][1], Bh[0][1], b_addr[0]);          \
    lds_tr_issue<((b) * 2 + (h)) * HT + 4096>(Bl[1][1], Bh[1][1], b_addr[1]);          \
  } while (0)
#define TIE_A()                                                                        \
  do {                                                                                 \
    _Pragma("unroll") for (int m = 0; m < 4; ++m) { opaque(At[m][0]); opaque(At[m][1]); } \
  } while (0)
#define TIE_B(Bl, Bh)                                                                  \
  do {                                                                                 \
    _Pragma("unroll") for (int n = 0; n < 2; ++n) { opaque(Bl[n][0]); opaque(Bl[n][1]); opaque(Bh[n][0]); opaque(Bh[n][1]); } \
  } while (0)
#define MMA_(ai, bj, Bl, Bh)                                                           \
  do {                                                                                 \
    __builtin_amdgcn_s_setprio(1);                                                     \
    _Pragma("unroll") for (int m = 0; m < 4; ++m)                                      \
    _Pragma("unroll") for (int n = 0; n < 2; ++n)                                      \
    _Pragma("unroll") for (int k = 0; k < 2; ++k)                                      \
      acc[ai][bj][m][n] = __builtin_amdgcn_mfma_f32_16x16x32_bf16(cat4(Bl[n][k], Bh[n][k]), At[m][k], acc[ai][bj][m][n], 0, 0, 0); \
    __builtin_amdgcn_s_setprio(0);                                                     \
  } while (0)
#define WAITV(n) asm volatile("s_waitcnt vmcnt(" #n ")" ::: "memory")
#define WAITL(n) asm volatile("s_waitcnt lgkmcnt(" #n ")" ::: "memory")
#define BAR_ __builtin_amdgcn_s_barrier()
#define SCHED_ __builtin_amdgcn_sched_barrier(0)
  STAGE_B(0, 0, 0); STAGE_A(0, 0, 0); STAGE_B(0, 1, 0); STAGE_A(0, 1, 0);
  if (wr == 1) BAR_;
  WAITV(4); BAR_;
  STAGE_B(1, 0, 1); STAGE_A(1, 0, 1); STAGE_B(1, 1, 1);
  WAITV(6); BAR_;
  for (int t = 0; t < nt - 2; t += 2) {
    LDB_(B0l, B0h, 0, 0); SCHED_; LDA_(0, 0); STAGE_A(1, 1, t + 1);
    WAITL(8); BAR_; WAITL(0); TIE_A(); TIE_B(B0l, B0h); MMA_(0, 0, B0l, B0h); BAR_; SCHED_;
    LDB_(B1l, B1h, 0, 1); STAGE_B(0, 0, t + 2);
    BAR_; WAITL(0); TIE_B(B1l, B1h); MMA_(0, 1, B1l, B1h); BAR_;
    LDA_(0, 1); STAGE_A(0, 0, t + 2);
    BAR_; WAITL(0); TIE_A(); MMA_(1, 0, B0l, B0h); BAR_; SCHED_;
    STAGE_B(0, 1, t + 2);
    WAITV(6); BAR_; MMA_(1, 1, B1l, B1h); BAR_;
    LDB_(B0l, B0h, 1, 0); SCHED_; LDA_(1, 0); STAGE_A(0, 1, t + 2);
    WAITL(8); BAR_; WAITL(0); TIE_A(); TIE_B(B0l, B0h); MMA_(0, 0, B0l, B0h); BAR_; SCHED_;
    LDB_(B1l, B1h, 1, 1); STAGE_B(1, 0, t + 3);
    BAR_; WAITL(0); TIE_B(B1l, B1h); MMA_(0, 1, B1l, B1h); BAR_;
    LDA_(1, 1); STAGE_A(1, 0, t + 3);
    BAR_; WAITL(0); TIE_A(); MMA_(1, 0, B0l, B0h); BAR_; SCHED_;
    STAGE_B(1, 1, t + 3);
    WAITV(6); BAR_; MMA_(1, 1, B1l, B1h); BAR_;
  }
  {
    LDB_(B0l, B0h, 0, 0); WAITV(0); LDA_(0, 0); STAGE_A(1, 1, nt - 1);
    BAR_; WAITL(0); TIE_A(); TIE_B(B0l, B0h); MMA_(0, 0, B0l, B0h); BAR_;
    LDB_(B1l, B1h, 0, 1); BAR_; WAITL(0); TIE_B(B1l, B1h); MMA_(0, 1, B1l, B1h); BAR_;
    LDA_(0, 1); WAITV(4); BAR_; WAITL(0); TIE_A(); MMA_(1, 0, B0l, B0h); MMA_(1, 1, B1l, B1h); BAR_;
  }
  {
    LDB_(B0l, B0h, 1, 0); LDA_(1, 0); WAITV(2); BAR_; WAITL(0); TIE_A(); TIE_B(B0l, B0h); MMA_(0, 0, B0l, B0h); BAR_;
    LDB_(B1l, B1h, 1, 1); WAITV(0); BAR_; WAITL(0); TIE_B(B1l, B1h); MMA_(0, 1, B1l, B1h); BAR_;
    LDA_(1, 1); BAR_; WAITL(0); TIE_A(); MMA_(1, 0, B0l, B0h); MMA_(1, 1, B1l, B1h); BAR_;
  }
  if (wr == 0) BAR_;
#undef SA_
#undef SB_
#undef STAGE_A
#undef STAGE_B
#undef LDA_
#undef LDB_
#undef TIE_A
#undef TIE_B
#undef MMA_
}

__global__ __launch_bounds__(512, 2) void gemm_gu8_kernel(const u16* __restrict__ xn, const u16* __restrict__ w1,
                                                          const u16* __restrict__ w3, u16* __restrict__ h) {
  __shared__ __attribute__((aligned(16))) char smem[8 * 16384];
  const int lane = threadIdx.x & 63, wid = threadIdx.x >> 6;
  const int L = (blockIdx.x & 7) * 28 + (blockIdx.x >> 3);
  const int colb = L >> 3, rowt = L & 7;
  const int row0 = rowt * 256, f0 = colb * 128;
  int krow;
  const int lc = bchunk_col(lane, wid, krow);
  const int ntl = lc >> 4;
  const u16* bp = ((ntl & 1) ? w3 : w1) + (size_t)krow * F_ + f0 + (ntl >> 1) * 16 + (lc & 15);
  f32x4 acc[2][2][4][2];
#pragma unroll
  for (int i = 0; i < 2; ++i)
#pragma unroll
    for (int j = 0; j < 2; ++j)
#pragma unroll
      for (int m = 0; m < 4; ++m)
#pragma unroll
        for (int n = 0; n < 2; ++n) acc[i][j][m][n] = (f32x4){0.f, 0.f, 0.f, 0.f};
  gemm256_8ph(xn + (size_t)row0 * D_, D_, bp, 32, F_, 16, smem, acc);
  int tz = threadIdx.x;
  asm volatile("" : "+v"(tz));
  const int l2 = tz & 63, w2i = tz >> 6, fr = l2 & 15, fq = l2 >> 4, wr = w2i >> 2, wc = w2i & 3;
#pragma unroll
  for (int ai = 0; ai < 2; ++ai)
#pragma unroll
    for (int bj = 0; bj < 2; ++bj) {
      const int col = f0 + bj * 64 + wc * 16 + fq * 4;
#pragma unroll
      for (int m = 0; m < 4; ++m) {
        const int row = row0 + ai * 128 + wr * 64 + m * 16 + fr;
        s16x4 o;
#pragma unroll
        for (int r = 0; r < 4; ++r) {
          const float g = rbf(acc[ai][bj][m][0][r]);
          const float u = rbf(acc[ai][bj][m][1][r]);
          o[r] = (short)f2bf(rbf(silu_f(g)) * u);
        }
        *reinterpret_cast<s16x4*>(h + (size_t)row * F_ + col) = o;
      }
    }
}

typedef GemmCfg<256, 256, 2, 4> CfgGU;
__global__ __launch_bounds__(512, 2) void gemm_gu_kernel(const u16* __restrict__ xn, const u16* __restrict__ w1,
                                                         const u16* __restrict__ w3, u16* __restrict__ h) {
  __shared__ __attribute__((aligned(16))) char smem[CfgGU::LDS];
  const int lane = threadIdx.x & 63, wid = threadIdx.x >> 6;
  const int L = (blockIdx.x & 7) * 28 + (blockIdx.x >> 3);
  const int colb = L >> 3, rowt = L & 7;
  const int row0 = rowt * 256, f0 = colb * 128;
  int krow;
  const int lc = bchunk_col(lane, wid, krow);
  const int ntl = lc >> 4;
  const u16* bp = ((ntl & 1) ? w3 : w1) + (size_t)krow * F_ + f0 + (ntl >> 1) * 16 + (lc & 15);
  f32x4 acc[CfgGU::MT][CfgGU::NT];
#pragma unroll
  for (int m = 0; m < CfgGU::MT; ++m)
#pragma unroll
    for (int n = 0; n < CfgGU::NT; ++n) acc[m][n] = (f32x4){0.f, 0.f, 0.f, 0.f};
  gemm_mainloop<CfgGU>(xn + (size_t)row0 * D_, D_, bp, 32, F_, 16, smem, acc);
  int tz = threadIdx.x;
  asm volatile("" : "+v"(tz));
  const int l2 = tz & 63, w2i = tz >> 6, fr = l2 & 15, fq = l2 >> 4, wm = w2i >> 2, wn = w2i & 3;
#pragma unroll
  for (int n = 0; n < CfgGU::NT; n += 2) {
    const int col = f0 + wn * 32 + (n >> 1) * 16 + fq * 4;
#pragma unroll
    for (int m = 0; m < CfgGU::MT; ++m) {
      const int row = row0 + wm * 128 + m * 16 + fr;
      s16x4 o;
#pragma unroll
      for (int r = 0; r < 4; ++r) {
        const float g = rbf(acc[m][n][r]);
        const float u = rbf(acc[m][n + 1][r]);
        o[r] = (short)f2bf(rbf(silu_f(g)) * u);
      }
      *reinterpret_cast<s16x4*>(h + (size_t)row * F_ + col) = o;
    }
  }
}

typedef RingCfg<128, 192, 2, 4, 3> CfgQKV;
__global__ __launch_bounds__(512, 2) void gemm_qkv_kernel(const u16* __restrict__ xn, const u16* __restrict__ wqkv,
                                                          u16* __restrict__ qkv) {
  __shared__ __attribute__((aligned(16))) char smem[CfgQKV::LDS];
  const int lane = threadIdx.x & 63, wid = threadIdx.x >> 6;
  const int L = (blockIdx.x & 7) * 32 + (blockIdx.x >> 3);
  const int colb = L >> 4, rowt = L & 15;
  const int row0 = rowt * 128, col0 = colb * 192;
  int krow;
  const int lc = bchunk_col(lane, wid, krow);
  const u16* bp = wqkv + (size_t)krow * 3072 + col0 + lc;
  f32x4 acc[CfgQKV::MT][CfgQKV::NT];
#pragma unroll
  for (int m = 0; m < CfgQKV::MT; ++m)
#pragma unroll
    for (int n = 0; n < CfgQKV::NT; ++n) acc[m][n] = (f32x4){0.f, 0.f, 0.f, 0.f};
  gemm_ring<CfgQKV>(xn + (size_t)row0 * D_, D_, bp, 64, 3072, 16, smem, acc);
  int tz = threadIdx.x;
  asm volatile("" : "+v"(tz));
  const int l2 = tz & 63, w2i = tz >> 6, fr = l2 & 15, fq = l2 >> 4, wm = w2i >> 2, wn = w2i & 3;
#pragma unroll
  for (int n = 0; n < CfgQKV::NT; ++n) {
    const int col = col0 + wn * 48 + n * 16 + fq * 4;
#pragma unroll
    for (int m = 0; m < CfgQKV::MT; ++m) {
      const int row = row0 + wm * 64 + m * 16 + fr;
      s16x4 o;
#pragma unroll
      for (int r = 0; r < 4; ++r) o[r] = (short)f2bf(acc[m][n][r]);
      *reinterpret_cast<s16x4*>(qkv + (size_t)row * 3072 + col) = o;
    }
  }
}

__global__ __launch_bounds__(256) void attn_kernel(const u16* __restrict__ qkv, u16* __restrict__ o) {
  const int bl = (blockIdx.x & 7) * 128 + (blockIdx.x >> 3);
  const int idx = bl * 256 + threadIdx.x;
  const int c = idx & 3, hh = (idx >> 2) & 31, s = idx >> 7;
  const u16* base = qkv + hh * 32 + c * 8;
  bf16x8 qv = *reinterpret_cast<const bf16x8*>(base + (size_t)s * 3072);
  bf16x8 kv[8], vv[8];
#pragma unroll
  for (int j = 0; j < 8; ++j) {
    const int pos = max(s - 2 * j, 0);
    kv[j] = *reinterpret_cast<const bf16x8*>(base + (size_t)pos * 3072 + 1024);
    vv[j] = *reinterpret_cast<const bf16x8*>(base + (size_t)pos * 3072 + 2048);
  }
  float qf[8];
#pragma unroll
  for (int i = 0; i < 8; ++i) qf[i] = bf2f((u16)qv[i]);
  float lg[8];
  const float scale = 0.17677669529663687f;
#pragma unroll
  for (int j = 0; j < 8; ++j) {
    float d = 0.f;
#pragma unroll
    for (int i = 0; i < 8; ++i) d += qf[i] * bf2f((u16)kv[j][i]);
    d += __shfl_xor(d, 1);
    d += __shfl_xor(d, 2);
    lg[j] = (s - 2 * j >= 0) ? rbf(rbf(d) * scale) : -1e9f;
  }
  float mx = lg[0];
#pragma unroll
  for (int j = 1; j < 8; ++j) mx = fmaxf(mx, lg[j]);
  float e[8], sum = 0.f;
#pragma unroll
  for (int j = 0; j < 8; ++j) { e[j] = __expf(lg[j] - mx); sum += e[j]; }
  const float inv = 1.0f / sum;
  float of[8];
#pragma unroll
  for (int i = 0; i < 8; ++i) of[i] = 0.f;
#pragma unroll
  for (int j = 0; j < 8; ++j) {
    const float a = rbf(e[j] * inv);
#pragma unroll
    for (int i = 0; i < 8; ++i) of[i] += a * bf2f((u16)vv[j][i]);
  }
  bf16x8 ov;
#pragma unroll
  for (int i = 0; i < 8; ++i) ov[i] = (short)f2bf(of[i]);
  *reinterpret_cast<bf16x8*>(o + (size_t)s * D_ + hh * 32 + c * 8) = ov;
}

constexpr int G2_ASZ = 128 * 128;
constexpr int G2_BSZ = 2 * 8192;
constexpr int G2_STAGE = G2_ASZ + G2_BSZ;

__device__ __forceinline__ void gemm2_loop(const u16* __restrict__ A, int lda, const u16* __restrict__ B,
                                           int row0, int col0, int k0, int nkt, char* smem, f32x4 (&acc)[4][4]) {
  const int tid = threadIdx.x, lane = tid & 63, wid = tid >> 6;
  const int wr = wid >> 1, wc = wid & 1;
  const int fr = lane & 15, fq = lane >> 4;
  const u16* asrc[4];
#pragma unroll
  for (int j = 0; j < 4; ++j) {
    const int r = wid * 32 + j * 8 + (lane >> 3);
    const int c = (lane & 7) ^ ((r >> 1) & 7);
    asrc[j] = A + (size_t)(row0 + r) * lda + k0 + c * 8;
  }
  const u16* bsrc[2][2];
#pragma unroll
  for (int j = 0; j < 2; ++j) {
    const int k = wid * 16 + j * 8 + (lane >> 3);
    const int p16 = lane & 7;
    const int g = ((k >> 1) & 1) | (((k >> 3) & 1) << 1);
    const int c = (p16 >> 1) ^ g, half = p16 & 1;
#pragma unroll
    for (int s = 0; s < 2; ++s) bsrc[s][j] = B + (size_t)(k0 + k) * D_ + col0 + s * 64 + c * 16 + half * 8;
  }
  auto stage = [&](int kt, int buf) {
    char* sa = smem + buf * G2_STAGE;
    char* sb = sa + G2_ASZ;
#pragma unroll
    for (int j = 0; j < 4; ++j) glds16(asrc[j] + kt * 64, sa + (wid * 32 + j * 8) * 128 + lane * 16);
#pragma unroll
    for (int s = 0; s < 2; ++s)
#pragma unroll
      for (int j = 0; j < 2; ++j)
        glds16(bsrc[s][j] + (size_t)kt * 64 * D_, sb + s * 8192 + (wid * 16 + j * 8) * 128 + lane * 16);
  };
  const int aswz = (fr >> 1) & 7;
  const int q4 = (lane & 15) >> 2, p4 = lane & 3;
  const int bswz = (q4 >> 1) | ((fq & 1) << 1);

  const unsigned sbase = lds_addr(smem);
  unsigned a_addr[2], b_addr[4];
#pragma unroll
  for (int kk = 0; kk < 2; ++kk) a_addr[kk] = sbase + (wr * 64 + fr) * 128 + (((kk * 4 + fq) ^ aswz) * 16);
#pragma unroll
  for (int n = 0; n < 4; ++n) {
    const int nt = wc * 4 + n;
    b_addr[n] = sbase + G2_ASZ + (nt >> 2) * 8192 + (fq * 8 + q4) * 128 + (((nt & 3) ^ bswz) * 32) + p4 * 8;
  }
  stage(0, 0);
  asm volatile("s_waitcnt vmcnt(0)" ::: "memory");
  __syncthreads();
  for (int kt = 0; kt < nkt; ++kt) {
    const int buf = kt & 1;
    if (kt + 1 < nkt) stage(kt + 1, buf ^ 1);
    const unsigned so = buf * G2_STAGE;
#pragma unroll
    for (int kk = 0; kk < 2; ++kk) {
      s16x4 bl[4], bh[4];
      bf16x8 a[4];
#pragma unroll
      for (int n = 0; n < 4; ++n) {
        if (kk == 0) lds_tr_issue<0>(bl[n], bh[n], b_addr[n] + so);
        else lds_tr_issue<4096>(bl[n], bh[n], b_addr[n] + so);
      }
      const unsigned aa = a_addr[kk] + so;
      lds_b128_issue<0 * 2048>(a[0], aa);
      lds_b128_issue<1 * 2048>(a[1], aa);
      lds_b128_issue<2 * 2048>(a[2], aa);
      lds_b128_issue<3 * 2048>(a[3], aa);
      asm volatile("s_waitcnt lgkmcnt(0)"
                   : "+v"(bl[0]), "+v"(bh[0]), "+v"(bl[1]), "+v"(bh[1]), "+v"(bl[2]), "+v"(bh[2]), "+v"(bl[3]),
                     "+v"(bh[3]), "+v"(a[0]), "+v"(a[1]), "+v"(a[2]), "+v"(a[3])::"memory");
#pragma unroll
      for (int m = 0; m < 4; ++m)
#pragma unroll
        for (int n = 0; n < 4; ++n)
          acc[m][n] = __builtin_amdgcn_mfma_f32_16x16x32_bf16(cat4(bl[n], bh[n]), a[m], acc[m][n], 0, 0, 0);
    }
    asm volatile("s_waitcnt vmcnt(0)" ::: "memory");
    __syncthreads();
  }
}

typedef RingCfg<128, 128, 2, 2, 4> CfgG2;
__global__ __launch_bounds__(256, 1) void gemm2_kernel(const u16* __restrict__ o, const u16* __restrict__ wo,
                                                       const u16* __restrict__ h, const u16* __restrict__ w2,
                                                       u16* __restrict__ sbuf, float* __restrict__ g0,
                                                       float* __restrict__ g1) {
  __shared__ __attribute__((aligned(16))) char smem[CfgG2::LDS];
  const int tid = threadIdx.x, lane = tid & 63, wid = tid >> 6;
  const int wr = wid >> 1, wc = wid & 1;
  const int fr = lane & 15, fq = lane >> 4;
  const int xcd = blockIdx.x & 7, idx = blockIdx.x >> 3;
  const int split = idx & 1;
  const int t = idx >> 1;
  const int tn = (xcd & 1) * 4 + (t & 3);
  const int tm = (xcd >> 1) * 4 + (t >> 2);
  const int row0 = tm * 128, col0 = tn * 128;
  const int krow = wid * 16 + (lane >> 3);
  const int g = ((krow >> 1) & 1) | (((krow >> 3) & 1) << 1);
  const int lc = (((lane & 7) >> 1) ^ g) * 16 + (lane & 1) * 8;
  f32x4 acc[4][4];
#pragma unroll
  for (int m = 0; m < 4; ++m)
#pragma unroll
    for (int n = 0; n < 4; ++n) acc[m][n] = (f32x4){0.f, 0.f, 0.f, 0.f};
  if (split == 0) {
    gemm_ring<CfgG2>(o + (size_t)row0 * D_, D_, wo + (size_t)krow * D_ + col0 + lc, 64, D_, 16, smem, acc, (lc ^ 32) - lc);
#pragma unroll
    for (int m = 0; m < 4; ++m)
#pragma unroll
      for (int n = 0; n < 4; ++n) {
        const int row = row0 + wr * 64 + m * 16 + fr, col = col0 + wc * 64 + n * 16 + fq * 4;
        s16x4 ov;
#pragma unroll
        for (int r = 0; r < 4; ++r) ov[r] = (short)f2bf(acc[m][n][r]);
        *reinterpret_cast<s16x4*>(sbuf + (size_t)row * D_ + col) = ov;
        acc[m][n] = (f32x4){0.f, 0.f, 0.f, 0.f};
      }
    gemm_ring<CfgG2>(h + (size_t)row0 * F_, F_, w2 + (size_t)krow * D_ + col0 + lc, 64, D_, 20, smem, acc, (lc ^ 32) - lc);
  } else {
    gemm_ring<CfgG2>(h + (size_t)row0 * F_ + 1280, F_, w2 + (size_t)(1280 + krow) * D_ + col0 + lc, 64, D_, 36, smem, acc, (lc ^ 32) - lc);
  }
  float* gd = split == 0 ? g0 : g1;
#pragma unroll
  for (int m = 0; m < 4; ++m)
#pragma unroll
    for (int n = 0; n < 4; ++n) {
      const int row = row0 + wr * 64 + m * 16 + fr, col = col0 + wc * 64 + n * 16 + fq * 4;
      *reinterpret_cast<f32x4*>(gd + (size_t)row * D_ + col) = acc[m][n];
    }
}

__global__ __launch_bounds__(256) void final_kernel(const u16* __restrict__ x, const u16* __restrict__ sbuf,
                                                    const float* __restrict__ g0, const float* __restrict__ g1,
                                                    u16* __restrict__ out) {
  const size_t i = ((size_t)blockIdx.x * 256 + threadIdx.x) * 8;
  bf16x8 xv = *reinterpret_cast<const bf16x8*>(x + i);
  bf16x8 sv = *reinterpret_cast<const bf16x8*>(sbuf + i);
  f32x4 a0 = *reinterpret_cast<const f32x4*>(g0 + i), a1 = *reinterpret_cast<const f32x4*>(g0 + i + 4);
  f32x4 b0 = *reinterpret_cast<const f32x4*>(g1 + i), b1 = *reinterpret_cast<const f32x4*>(g1 + i + 4);
  bf16x8 ov;
#pragma unroll
  for (int k = 0; k < 8; ++k) {
    const float g = rbf(k < 4 ? a0[k] + b0[k] : a1[k - 4] + b1[k - 4]);
    const float sg = rbf(silu_f(g));
    const float p = rbf(bf2f((u16)sv[k]) * sg);
    ov[k] = (short)f2bf(bf2f((u16)xv[k]) + p);
  }
  *reinterpret_cast<bf16x8*>(out + i) = ov;
}

extern "C" void kernel_launch(void* const* d_in, const int* in_sizes, int n_in, void* d_out, int out_size,
                              void* d_ws, size_t ws_size, hipStream_t stream) {
  const u16* x = (const u16*)d_in[0];
  const u16* wqkv = (const u16*)d_in[1];
  const u16* wo = (const u16*)d_in[2];
  const u16* w1 = (const u16*)d_in[4];
  const u16* w3 = (const u16*)d_in[5];
  const u16* w2 = (const u16*)d_in[6];
  u16* out = (u16*)d_out;
  char* ws = (char*)d_ws;
  u16* xn = (u16*)ws;
  u16* qkv = (u16*)(ws + (4u << 20));
  u16* h = (u16*)(ws + (16u << 20));
  u16* o = (u16*)(ws + (32u << 20));
  u16* sbuf = (u16*)(ws + (36u << 20));
  float* g0 = (float*)(ws + (40u << 20));
  float* g1 = (float*)(ws + (48u << 20));

  ln_kernel<<<S_ / 4, 256, 0, stream>>>(x, xn);
  gemm_gu8_kernel<<<224, 512, 0, stream>>>(xn, w1, w3, h);
  gemm_qkv_kernel<<<256, 512, 0, stream>>>(xn, wqkv, qkv);
  attn_kernel<<<S_ * NH * 4 / 256, 256, 0, stream>>>(qkv, o);
  gemm2_kernel<<<256, 256, 0, stream>>>(o, wo, h, w2, sbuf, g0, g1);
  final_kernel<<<S_ * D_ / 8 / 256, 256, 0, stream>>>(x, sbuf, g0, g1, out);
}
```

```cpp
#include <hip/hip_runtime.h>
#include <hip/hip_bf16.h>
#include <stdint.h>
#include <type_traits>

typedef __attribute__((ext_vector_type(8))) short bf16x8;
typedef __attribute__((ext_vector_type(4))) short s16x4;
typedef __attribute__((ext_vector_type(4))) float f32x4;
typedef unsigned short u16;
typedef __attribute__((address_space(3))) s16x4* lds_s16x4_p;

constexpr int S_ = 2048, D_ = 1024, F_ = 3584, NH = 32, DH = 32;

__device__ __forceinline__ u16 f2bf(float f) {
  uint32_t u = __float_as_uint(f);
  u += 0x7fffu + ((u >> 16) & 1u);
  return (u16)(u >> 16);
}
__device__ __forceinline__ float bf2f(u16 h) { return __uint_as_float(((uint32_t)h) << 16); }
__device__ __forceinline__ float rbf(float f) { return bf2f(f2bf(f)); }
__device__ __forceinline__ float silu_f(float v) { return v / (1.0f + __expf(-v)); }

__device__ __forceinline__ void glds16(const void* g, void* l) {
  __builtin_amdgcn_global_load_lds((const __attribute__((address_space(1))) unsigned*)g,
                                   (__attribute__((address_space(3))) unsigned*)l, 16, 0, 0);
}
__device__ __forceinline__ bf16x8 lds_read128(const char* p) {
  return *reinterpret_cast<const bf16x8*>(p);
}
template <int OFF>
__device__ __forceinline__ void lds_tr_issue(s16x4& a, s16x4& b, unsigned addr) {
  asm volatile("ds_read_b64_tr_b16 %0, %2 offset:%3\n\tds_read_b64_tr_b16 %1, %2 offset:%4"
               : "=&v"(a), "=&v"(b)
               : "v"(addr), "i"(OFF), "i"(OFF + 512)
               : "memory");
}
template <int OFF>
__device__ __forceinline__ void lds_b128_issue(bf16x8& a, unsigned addr) {
  asm volatile("ds_read_b128 %0, %1 offset:%2" : "=&v"(a) : "v"(addr), "i"(OFF) : "memory");
}
template <int I, int MT, int OFF>
__device__ __forceinline__ void lds_a_issue_rec(bf16x8 (&a)[MT], unsigned addr) {
  if constexpr (I < MT) {
    lds_b128_issue<OFF + I * 2048>(a[I], addr);
    lds_a_issue_rec<I + 1, MT, OFF>(a, addr);
  }
}
template <int MT, int OFF>
__device__ __forceinline__ void lds_a_issue(bf16x8 (&a)[MT], unsigned addr) { lds_a_issue_rec<0, MT, OFF>(a, addr); }
__device__ __forceinline__ void opaque(s16x4& v) { asm volatile("" : "+v"(v)); }
__device__ __forceinline__ void opaque(bf16x8& v) { asm volatile("" : "+v"(v)); }
template <int NT, int MT>
__device__ __forceinline__ void lds_wait_all(s16x4 (&bl)[NT], s16x4 (&bh)[NT], bf16x8 (&a)[MT]) {
  asm volatile("s_waitcnt lgkmcnt(0)" ::: "memory");
#pragma unroll
  for (int n = 0; n < NT; ++n) { opaque(bl[n]); opaque(bh[n]); }
#pragma unroll
  for (int m = 0; m < MT; ++m) opaque(a[m]);
}
__device__ __forceinline__ bf16x8 cat4(s16x4 a, s16x4 b) {
  return __builtin_shufflevector(a, b, 0, 1, 2, 3, 4, 5, 6, 7);
}
__device__ __forceinline__ unsigned lds_addr(const void* p) {
  return (unsigned)(uintptr_t)(const __attribute__((address_space(3))) char*)p;
}

__global__ __launch_bounds__(256) void ln_kernel(const u16* __restrict__ x, u16* __restrict__ xn) {
  const int lane = threadIdx.x & 63;
  const int row = blockIdx.x * 4 + (threadIdx.x >> 6);
  const u16* xr = x + (size_t)row * D_;
  bf16x8 v0 = *reinterpret_cast<const bf16x8*>(xr + lane * 8);
  bf16x8 v1 = *reinterpret_cast<const bf16x8*>(xr + 512 + lane * 8);
  float f[16];
#pragma unroll
  for (int i = 0; i < 8; ++i) { f[i] = bf2f((u16)v0[i]); f[8 + i] = bf2f((u16)v1[i]); }
  float s = 0.f;
#pragma unroll
  for (int i = 0; i < 16; ++i) s += f[i];
#pragma unroll
  for (int o = 32; o >= 1; o >>= 1) s += __shfl_xor(s, o);
  const float mean = s * (1.0f / D_);
  float q = 0.f;
#pragma unroll
  for (int i = 0; i < 16; ++i) { float d = f[i] - mean; q += d * d; }
#pragma unroll
  for (int o = 32; o >= 1; o >>= 1) q += __shfl_xor(q, o);
  const float var = q * (1.0f / D_);
  const float mb = rbf(mean);
  const float vb = rbf(var);
  const float den = rbf(sqrtf(rbf(vb + 1e-5f)));
  bf16x8 o0, o1;
#pragma unroll
  for (int i = 0; i < 8; ++i) {
    o0[i] = (short)f2bf(rbf(f[i] - mb) / den);
    o1[i] = (short)f2bf(rbf(f[8 + i] - mb) / den);
  }
  u16* orow = xn + (size_t)row * D_;
  *reinterpret_cast<bf16x8*>(orow + lane * 8) = o0;
  *reinterpret_cast<bf16x8*>(orow + 512 + lane * 8) = o1;
}

template <int BM, int BN, int WAVES_M, int WAVES_N>
struct GemmCfg {
  static constexpr int NWN = WAVES_N;
  static constexpr int WM = BM / WAVES_M, WN = BN / WAVES_N;
  static constexpr int MT = WM / 16, NT = WN / 16;
  static constexpr int ASZ = BM * 128, BSZ = (BN / 64) * 8192;
  static constexpr int LDS = 2 * (ASZ + BSZ);
  static constexpr int AI = BM / 64;
  static constexpr int BI = BN / 64;
};

template <class C>
__device__ __forceinline__ void gemm_mainloop(const u16* __restrict__ aptr, int lda, const u16* __restrict__ bptr,
                                              int bsub, int ldb, int nkt, char* smem,
                                              f32x4 (&acc)[C::MT][C::NT]) {
  const int tid = threadIdx.x, lane = tid & 63, wid = tid >> 6;
  const int wu = __builtin_amdgcn_readfirstlane(wid);
  const int wm = wu / C::NWN, wn = wu % C::NWN;
  const int fr = lane & 15, fq = lane >> 4;
  unsigned aoff[C::AI];
#pragma unroll
  for (int j = 0; j < C::AI; ++j) {
    const int r = wu * (C::AI * 8) + j * 8 + (lane >> 3);
    const int c = (lane & 7) ^ ((r >> 1) & 7);
    aoff[j] = (unsigned)(r * lda + c * 8) * 2u;
  }
  const char* abase = (const char*)aptr;
  auto stage = [&](int kt, int buf) {
    char* sa = smem + buf * C::ASZ + wu * (C::AI * 1024);
    char* sb = smem + 2 * C::ASZ + buf * C::BSZ + wu * 1024;
#pragma unroll
    for (int j = 0; j < C::AI; ++j) glds16(abase + kt * 128 + aoff[j], sa + j * 1024);
    const u16* bp = bptr + (size_t)kt * 64 * ldb;
#pragma unroll
    for (int s = 0; s < C::BI; ++s) glds16(bp + s * bsub, sb + s * 8192);
  };
  const int aswz = (fr >> 1) & 7;
  const int q4 = (lane & 15) >> 2, p4 = lane & 3;
  const int bswz = (q4 >> 1) | ((fq & 1) << 1);
  const unsigned sbase = lds_addr(smem);
  unsigned a_addr[2], b_addr[C::NT];
#pragma unroll
  for (int kk = 0; kk < 2; ++kk) a_addr[kk] = sbase + (wm * C::WM + fr) * 128 + (((kk * 4 + fq) ^ aswz) * 16);
#pragma unroll
  for (int n = 0; n < C::NT; ++n) {
    const int nt = wn * C::NT + n;
    b_addr[n] = sbase + 2 * C::ASZ + (nt >> 2) * 8192 + (fq * 8 + q4) * 128 + (((nt & 3) ^ bswz) * 32) + p4 * 8;
  }
  auto compute = [&](auto bufc) {
    constexpr int BUF = decltype(bufc)::value;
#pragma unroll
    for (int kk = 0; kk < 2; ++kk) {
      s16x4 bl[C::NT], bh[C::NT];
      bf16x8 a[C::MT];
#pragma unroll
      for (int n = 0; n < C::NT; ++n) {
        if (kk == 0) lds_tr_issue<BUF * C::BSZ>(bl[n], bh[n], b_addr[n]);
        else lds_tr_issue<BUF * C::BSZ + 4096>(bl[n], bh[n], b_addr[n]);
      }
      lds_a_issue<C::MT, BUF * C::ASZ>(a, a_addr[kk]);
      lds_wait_all<C::NT, C::MT>(bl, bh, a);
#pragma unroll
      for (int m = 0; m < C::MT; ++m)
#pragma unroll
        for (int n = 0; n < C::NT; ++n)
          acc[m][n] = __builtin_amdgcn_mfma_f32_16x16x32_bf16(cat4(bl[n], bh[n]), a[m], acc[m][n], 0, 0, 0);
    }
  };
  stage(0, 0);
  asm volatile("s_waitcnt vmcnt(0)" ::: "memory");
  __builtin_amdgcn_s_barrier();
  for (int kt = 0; kt < nkt; kt += 2) {
    stage(kt + 1, 1);
    compute(std::integral_constant<int, 0>{});
    asm volatile("s_waitcnt vmcnt(0)" ::: "memory");
    __builtin_amdgcn_s_barrier();
    if (kt + 2 < nkt) stage(kt + 2, 0);
    compute(std::integral_constant<int, 1>{});
    asm volatile("s_waitcnt vmcnt(0)" ::: "memory");
    __builtin_amdgcn_s_barrier();
  }
}

#define BN_SUBS(C) (C::BSZ / 8192)
template <int N>
__device__ __forceinline__ void wait_vm() { asm volatile("s_waitcnt vmcnt(%0)" ::"i"(N) : "memory"); }
template <int N>
__device__ __forceinline__ void wait_lgkm() { asm volatile("s_waitcnt lgkmcnt(%0)" ::"i"(N) : "memory"); }
template <int BM, int BN, int WAVES_M, int WAVES_N, int NST_>
struct RingCfg {
  static constexpr int NW = WAVES_M * WAVES_N, NWN = WAVES_N, NST = NST_;
  static constexpr int WM = BM / WAVES_M, WN = BN / WAVES_N;
  static constexpr int MT = WM / 16, NT = WN / 16;
  static constexpr int ASZ = BM * 128, BSZ = (BN / 64) * 8192, STG = ASZ + BSZ;
  static constexpr int LDS = NST * STG;
  static constexpr int AI = BM / 8 / NW;
  static constexpr int BR = 64 / NW / 8;
  static constexpr int BI = (BN / 64) * BR;
  static constexpr int G = AI + BI;
};
template <class C>
__device__ __forceinline__ void gemm_ring(const u16* __restrict__ aptr, int lda, const u16* __restrict__ bptr,
                                          int bsub, int ldb, int nkt, char* smem, f32x4 (&acc)[C::MT][C::NT],
                                          int bodd = 0) {
  const int tid = threadIdx.x, lane = tid & 63;
  const int wu = __builtin_amdgcn_readfirstlane(tid >> 6);
  const int wm = wu / C::NWN, wn = wu % C::NWN;
  const int fr = lane & 15, fq = lane >> 4;
  unsigned aoff[C::AI];
#pragma unroll
  for (int j = 0; j < C::AI; ++j) {
    const int r = wu * (C::AI * 8) + j * 8 + (lane >> 3);
    const int c = (lane & 7) ^ ((r >> 1) & 7);
    aoff[j] = (unsigned)(r * lda + c * 8) * 2u;
  }
  const char* abase = (const char*)aptr;
  auto stage = [&](int kt, int buf) {
    char* sa = smem + buf * C::STG + wu * (C::AI * 1024);
    char* sb = smem + buf * C::STG + C::ASZ + wu * (C::BR * 1024);
#pragma unroll
    for (int j = 0; j < C::AI; ++j) glds16(abase + kt * 128 + aoff[j], sa + j * 1024);
    const u16* bp = bptr + (size_t)kt * 64 * ldb;
#pragma unroll
    for (int s = 0; s < BN_SUBS(C); ++s)
#pragma unroll
      for (int j = 0; j < C::BR; ++j) glds16(bp + s * bsub + (size_t)j * 8 * ldb + ((j & 1) ? bodd : 0), sb + s * 8192 + j * 1024);
  };
  const int aswz = (fr >> 1) & 7;
  const int q4 = (lane & 15) >> 2, p4 = lane & 3;
  const int bswz = (q4 >> 1) | ((fq & 1) << 1);
  const unsigned sbase = lds_addr(smem);
  unsigned a_addr[2], b_addr[C::NT];
#pragma unroll
  for (int kk = 0; kk < 2; ++kk) a_addr[kk] = sbase + (wm * C::WM + fr) * 128 + (((kk * 4 + fq) ^ aswz) * 16);
#pragma unroll
  for (int n = 0; n < C::NT; ++n) {
    const int nt = wn * C::NT + n;
    b_addr[n] = sbase + C::ASZ + (nt >> 2) * 8192 + (fq * 8 + q4) * 128 + (((nt & 3) ^ bswz) * 32) + p4 * 8;
  }
#pragma unroll
  for (int i = 0; i < C::NST - 1; ++i)
    if (i < nkt) stage(i, i);
  int cur = 0, nxt = C::NST - 1;
  for (int kt = 0; kt < nkt; ++kt) {
    if (kt + C::NST - 2 < nkt) wait_vm<(C::NST - 2) * C::G>();
    else wait_vm<0>();
    __builtin_amdgcn_s_barrier();
    if (kt + C::NST - 1 < nkt) stage(kt + C::NST - 1, nxt);
    const unsigned bo = cur * C::STG;
    s16x4 bl[2][C::NT], bh[2][C::NT];
    bf16x8 a[2][C::MT];
#pragma unroll
    for (int n = 0; n < C::NT; ++n) lds_tr_issue<0>(bl[0][n], bh[0][n], b_addr[n] + bo);
    lds_a_issue<C::MT, 0>(a[0], a_addr[0] + bo);
#pragma unroll
    for (int n = 0; n < C::NT; ++n) lds_tr_issue<4096>(bl[1][n], bh[1][n], b_addr[n] + bo);
    lds_a_issue<C::MT, 0>(a[1], a_addr[1] + bo);
    wait_lgkm<2 * C::NT + C::MT>();
#pragma unroll
    for (int n = 0; n < C::NT; ++n) { opaque(bl[0][n]); opaque(bh[0][n]); }
#pragma unroll
    for (int m = 0; m < C::MT; ++m) opaque(a[0][m]);
#pragma unroll
    for (int m = 0; m < C::MT; ++m)
#pragma unroll
      for (int n = 0; n < C::NT; ++n)
        acc[m][n] = __builtin_amdgcn_mfma_f32_16x16x32_bf16(cat4(bl[0][n], bh[0][n]), a[0][m], acc[m][n], 0, 0, 0);
    wait_lgkm<0>();
#pragma unroll
    for (int n = 0; n < C::NT; ++n) { opaque(bl[1][n]); opaque(bh[1][n]); }
#pragma unroll
    for (int m = 0; m < C::MT; ++m) opaque(a[1][m]);
#pragma unroll
    for (int m = 0; m < C::MT; ++m)
#pragma unroll
      for (int n = 0; n < C::NT; ++n)
        acc[m][n] = __builtin_amdgcn_mfma_f32_16x16x32_bf16(cat4(bl[1][n], bh[1][n]), a[1][m], acc[m][n], 0, 0, 0);
    cur = (cur + 1 == C::NST) ? 0 : cur + 1;
    nxt = (nxt + 1 == C::NST) ? 0 : nxt + 1;
  }
  __builtin_amdgcn_s_barrier();
}

__device__ __forceinline__ int bchunk_col(int lane, int wid, int& krow) {
  krow = wid * 8 + (lane >> 3);
  const int p16 = lane & 7;
  const int g = ((krow >> 1) & 1) | (((krow >> 3) & 1) << 1);
  const int c = (p16 >> 1) ^ g, half = p16 & 1;
  return c * 16 + half * 8;
}

__device__ __forceinline__ void gemm256_8ph(const u16* __restrict__ aptr, int lda, const u16* __restrict__ bptr,
                                            int bsub, int ldb, int nt, char* smem, f32x4 (&acc)[2][2][4][2]) {
  const int tid = threadIdx.x, lane = tid & 63;
  const int wu = __builtin_amdgcn_readfirstlane(tid >> 6);
  const int wr = wu >> 2, wc = wu & 3;
  const int fr = lane & 15, fq = lane >> 4;
  constexpr int HT = 16384;
  unsigned aoff[2];
#pragma unroll
  for (int j = 0; j < 2; ++j) {
    const int r = wu * 16 + j * 8 + (lane >> 3);
    const int c = (lane & 7) ^ ((r >> 1) & 7);
    aoff[j] = (unsigned)(r * lda + c * 8) * 2u;
  }
  const char* abase = (const char*)aptr;
  const size_t ahalf = (size_t)128 * lda * 2;
#define SA_(b, h) (smem + ((b) * 2 + (h)) * HT)
#define SB_(b, h) (smem + 4 * HT + ((b) * 2 + (h)) * HT)
#define STAGE_A(b, h, kt)                                                              \
  do {                                                                                 \
    const char* _g = abase + (h) * ahalf + (size_t)(kt) * 128;                         \
    glds16(_g + aoff[0], SA_(b, h) + wu * 2048);                                       \
    glds16(_g + aoff[1], SA_(b, h) + wu * 2048 + 1024);                                \
  } while (0)
#define STAGE_B(b, h, kt)                                                              \
  do {                                                                                 \
    const u16* _g = bptr + (size_t)(kt) * 64 * ldb + (h) * 2 * bsub;                   \
    glds16(_g, SB_(b, h) + wu * 1024);                                                 \
    glds16(_g + bsub, SB_(b, h) + 8192 + wu * 1024);                                   \
  } while (0)
  const int aswz = (fr >> 1) & 7;
  const int q4 = (lane & 15) >> 2, p4 = lane & 3;
  const int bswz = (q4 >> 1) | ((fq & 1) << 1);
  const unsigned sbase = lds_addr(smem);
  unsigned a_addr[2], b_addr[2];
#pragma unroll
  for (int kk = 0; kk < 2; ++kk) a_addr[kk] = sbase + (wr * 64 + fr) * 128 + (((kk * 4 + fq) ^ aswz) * 16);
#pragma unroll
  for (int n = 0; n < 2; ++n) {
    const int t = wc * 2 + n;
    b_addr[n] = sbase + 4 * HT + (t >> 2) * 8192 + (fq * 8 + q4) * 128 + (((t & 3) ^ bswz) * 32) + p4 * 8;
  }
  bf16x8 At[4][2];
  s16x4 B0l[2][2], B0h[2][2], B1l[2][2], B1h[2][2];
#define LDA_(b, h)                                                                     \
  do {                                                                                 \
    lds_b128_issue<((b) * 2 + (h)) * HT + 0 * 2048>(At[0][0], a_addr[0]);              \
    lds_b128_issue<((b) * 2 + (h)) * HT + 1 * 2048>(At[1][0], a_addr[0]);              \
    lds_b128_issue<((b) * 2 + (h)) * HT + 2 * 2048>(At[2][0], a_addr[0]);              \
    lds_b128_issue<((b) * 2 + (h)) * HT + 3 * 2048>(At[3][0], a_addr[0]);              \
    lds_b128_issue<((b) * 2 + (h)) * HT + 0 * 2048>(At[0][1], a_addr[1]);              \
    lds_b128_issue<((b) * 2 + (h)) * HT + 1 * 2048>(At[1][1], a_addr[1]);              \
    lds_b128_issue<((b) * 2 + (h)) * HT + 2 * 2048>(At[2][1], a_addr[1]);              \
    lds_b128_issue<((b) * 2 + (h)) * HT + 3 * 2048>(At[3][1], a_addr[1]);              \
  } while (0)
#define LDB_(Bl, Bh, b, h)                                                             \
  do {                                                                                 \
    lds_tr_issue<((b) * 2 + (h)) * HT>(Bl[0][0], Bh[0][0], b_addr[0]);                 \
    lds_tr_issue<((b) * 2 + (h)) * HT>(Bl[1][0], Bh[1][0], b_addr[1]);                 \
    lds_tr_issue<((b) * 2 + (h)) * HT + 4096>(Bl[0][1], Bh[0][1], b_addr[0]);          \
    lds_tr_issue<((b) * 2 + (h)) * HT + 4096>(Bl[1][1], Bh[1][1], b_addr[1]);          \
  } while (0)
#define TIE_A()                                                                        \
  do {                                                                                 \
    _Pragma("unroll") for (int m = 0; m < 4; ++m) { opaque(At[m][0]); opaque(At[m][1]); } \
  } while (0)
#define TIE_B(Bl, Bh)                                                                  \
  do {                                                                                 \
    _Pragma("unroll") for (int n = 0; n < 2; ++n) { opaque(Bl[n][0]); opaque(Bl[n][1]); opaque(Bh[n][0]); opaque(Bh[n][1]); } \
  } while (0)
#define MMA_(ai, bj, Bl, Bh)                                                           \
  do {                                                                                 \
    __builtin_amdgcn_s_setprio(1);                                                     \
    _Pragma("unroll") for (int m = 0; m < 4; ++m)                                      \
    _Pragma("unroll") for (int n = 0; n < 2; ++n)                                      \
    _Pragma("unroll") for (int k = 0; k < 2; ++k)                                      \
      acc[ai][bj][m][n] = __builtin_amdgcn_mfma_f32_16x16x32_bf16(cat4(Bl[n][k], Bh[n][k]), At[m][k], acc[ai][bj][m][n], 0, 0, 0); \
    __builtin_amdgcn_s_setprio(0);                                                     \
  } while (0)
#define WAITV(n) asm volatile("s_waitcnt vmcnt(" #n ")" ::: "memory")
#define WAITL(n) asm volatile("s_waitcnt lgkmcnt(" #n ")" ::: "memory")
#define BAR_ __builtin_amdgcn_s_barrier()
#define SCHED_ __builtin_amdgcn_sched_barrier(0)
  STAGE_B(0, 0, 0); STAGE_A(0, 0, 0); STAGE_B(0, 1, 0); STAGE_A(0, 1, 0);
  if (wr == 1) BAR_;
  WAITV(4); BAR_;
  STAGE_B(1, 0, 1); STAGE_A(1, 0, 1); STAGE_B(1, 1, 1);
  WAITV(6); BAR_;
  for (int t = 0; t < nt - 2; t += 2) {
    LDB_(B0l, B0h, 0, 0); SCHED_; LDA_(0, 0); STAGE_A(1, 1, t + 1);
    WAITL(8); BAR_; WAITL(0); TIE_A(); TIE_B(B0l, B0h); MMA_(0, 0, B0l, B0h); BAR_; SCHED_;
    LDB_(B1l, B1h, 0, 1); STAGE_B(0, 0, t + 2);
    BAR_; WAITL(0); TIE_B(B1l, B1h); MMA_(0, 1, B1l, B1h); BAR_;
    LDA_(0, 1); STAGE_A(0, 0, t + 2);
    BAR_; WAITL(0); TIE_A(); MMA_(1, 0, B0l, B0h); BAR_; SCHED_;
    STAGE_B(0, 1, t + 2);
    WAITV(6); BAR_; MMA_(1, 1, B1l, B1h); BAR_;
    LDB_(B0l, B0h, 1, 0); SCHED_; LDA_(1, 0); STAGE_A(0, 1, t + 2);
    WAITL(8); BAR_; WAITL(0); TIE_A(); TIE_B(B0l, B0h); MMA_(0, 0, B0l, B0h); BAR_; SCHED_;
    LDB_(B1l, B1h, 1, 1); STAGE_B(1, 0, t + 3);
    BAR_; WAITL(0); TIE_B(B1l, B1h); MMA_(0, 1, B1l, B1h); BAR_;
    LDA_(1, 1); STAGE_A(1, 0, t + 3);
    BAR_; WAITL(0); TIE_A(); MMA_(1, 0, B0l, B0h); BAR_; SCHED_;
    STAGE_B(1, 1, t + 3);
    WAITV(6); BAR_; MMA_(1, 1, B1l, B1h); BAR_;
  }
  {
    LDB_(B0l, B0h, 0, 0); WAITV(0); LDA_(0, 0); STAGE_A(1, 1, nt - 1);
    BAR_; WAITL(0); TIE_A(); TIE_B(B0l, B0h); MMA_(0, 0, B0l, B0h); BAR_;
    LDB_(B1l, B1h, 0, 1); BAR_; WAITL(0); TIE_B(B1l, B1h); MMA_(0, 1, B1l, B1h); BAR_;
    LDA_(0, 1); WAITV(4); BAR_; WAITL(0); TIE_A(); MMA_(1, 0, B0l, B0h); MMA_(1, 1, B1l, B1h); BAR_;
  }
  {
    LDB_(B0l, B0h, 1, 0); LDA_(1, 0); WAITV(2); BAR_; WAITL(0); TIE_A(); TIE_B(B0l, B0h); MMA_(0, 0, B0l, B0h); BAR_;
    LDB_(B1l, B1h, 1, 1); WAITV(0); BAR_; WAITL(0); TIE_B(B1l, B1h); MMA_(0, 1, B1l, B1h); BAR_;
    LDA_(1, 1); BAR_; WAITL(0); TIE_A(); MMA_(1, 0, B0l, B0h); MMA_(1, 1, B1l, B1h); BAR_;
  }
  if (wr == 0) BAR_;
#undef SA_
#undef SB_
#undef STAGE_A
#undef STAGE_B
#undef LDA_
#undef LDB_
#undef TIE_A
#undef TIE_B
#undef MMA_
}

__device__ __forceinline__ void gemm_gu8_body(int bid, char* smem, const u16* __restrict__ xn, const u16* __restrict__ w1,
                                              const u16* __restrict__ w3, u16* __restrict__ h) {
  const int lane = threadIdx.x & 63, wid = threadIdx.x >> 6;
  const int L = (bid & 7) * 28 + (bid >> 3);
  const int colb = L >> 3, rowt = L & 7;
  const int row0 = rowt * 256, f0 = colb * 128;
  int krow;
  const int lc = bchunk_col(lane, wid, krow);
  const int ntl = lc >> 4;
  const u16* bp = ((ntl & 1) ? w3 : w1) + (size_t)krow * F_ + f0 + (ntl >> 1) * 16 + (lc & 15);
  f32x4 acc[2][2][4][2];
#pragma unroll
  for (int i = 0; i < 2; ++i)
#pragma unroll
    for (int j = 0; j < 2; ++j)
#pragma unroll
      for (int m = 0; m < 4; ++m)
#pragma unroll
        for (int n = 0; n < 2; ++n) acc[i][j][m][n] = (f32x4){0.f, 0.f, 0.f, 0.f};
  gemm256_8ph(xn + (size_t)row0 * D_, D_, bp, 32, F_, 16, smem, acc);
  int tz = threadIdx.x;
  asm volatile("" : "+v"(tz));
  const int l2 = tz & 63, w2i = tz >> 6, fr = l2 & 15, fq = l2 >> 4, wr = w2i >> 2, wc = w2i & 3;
#pragma unroll
  for (int ai = 0; ai < 2; ++ai)
#pragma unroll
    for (int bj = 0; bj < 2; ++bj) {
      const int col = f0 + bj * 64 + wc * 16 + fq * 4;
#pragma unroll
      for (int m = 0; m < 4; ++m) {
        const int row = row0 + ai * 128 + wr * 64 + m * 16 + fr;
        s16x4 o;
#pragma unroll
        for (int r = 0; r < 4; ++r) {
          const float g = rbf(acc[ai][bj][m][0][r]);
          const float u = rbf(acc[ai][bj][m][1][r]);
          o[r] = (short)f2bf(rbf(silu_f(g)) * u);
        }
        *reinterpret_cast<s16x4*>(h + (size_t)row * F_ + col) = o;
      }
    }
}

typedef GemmCfg<256, 256, 2, 4> CfgGU;
__global__ __launch_bounds__(512, 2) void gemm_gu_kernel(const u16* __restrict__ xn, const u16* __restrict__ w1,
                                                         const u16* __restrict__ w3, u16* __restrict__ h) {
  __shared__ __attribute__((aligned(16))) char smem[CfgGU::LDS];
  const int lane = threadIdx.x & 63, wid = threadIdx.x >> 6;
  const int L = (blockIdx.x & 7) * 28 + (blockIdx.x >> 3);
  const int colb = L >> 3, rowt = L & 7;
  const int row0 = rowt * 256, f0 = colb * 128;
  int krow;
  const int lc = bchunk_col(lane, wid, krow);
  const int ntl = lc >> 4;
  const u16* bp = ((ntl & 1) ? w3 : w1) + (size_t)krow * F_ + f0 + (ntl >> 1) * 16 + (lc & 15);
  f32x4 acc[CfgGU::MT][CfgGU::NT];
#pragma unroll
  for (int m = 0; m < CfgGU::MT; ++m)
#pragma unroll
    for (int n = 0; n < CfgGU::NT; ++n) acc[m][n] = (f32x4){0.f, 0.f, 0.f, 0.f};
  gemm_mainloop<CfgGU>(xn + (size_t)row0 * D_, D_, bp, 32, F_, 16, smem, acc);
  int tz = threadIdx.x;
  asm volatile("" : "+v"(tz));
  const int l2 = tz & 63, w2i = tz >> 6, fr = l2 & 15, fq = l2 >> 4, wm = w2i >> 2, wn = w2i & 3;
#pragma unroll
  for (int n = 0; n < CfgGU::NT; n += 2) {
    const int col = f0 + wn * 32 + (n >> 1) * 16 + fq * 4;
#pragma unroll
    for (int m = 0; m < CfgGU::MT; ++m) {
      const int row = row0 + wm * 128 + m * 16 + fr;
      s16x4 o;
#pragma unroll
      for (int r = 0; r < 4; ++r) {
        const float g = rbf(acc[m][n][r]);
        const float u = rbf(acc[m][n + 1][r]);
        o[r] = (short)f2bf(rbf(silu_f(g)) * u);
      }
      *reinterpret_cast<s16x4*>(h + (size_t)row * F_ + col) = o;
    }
  }
}

typedef RingCfg<128, 192, 2, 4, 3> CfgQKV;
__device__ __forceinline__ void gemm_qkv_body(int bid, char* smem, const u16* __restrict__ xn,
                                              const u16* __restrict__ wqkv, u16* __restrict__ qkv) {
  const int lane = threadIdx.x & 63, wid = threadIdx.x >> 6;
  const int L = (bid & 7) * 32 + (bid >> 3);
  const int colb = L >> 4, rowt = L & 15;
  const int row0 = rowt * 128, col0 = colb * 192;
  int krow;
  const int lc = bchunk_col(lane, wid, krow);
  const u16* bp = wqkv + (size_t)krow * 3072 + col0 + lc;
  f32x4 acc[CfgQKV::MT][CfgQKV::NT];
#pragma unroll
  for (int m = 0; m < CfgQKV::MT; ++m)
#pragma unroll
    for (int n = 0; n < CfgQKV::NT; ++n) acc[m][n] = (f32x4){0.f, 0.f, 0.f, 0.f};
  gemm_ring<CfgQKV>(xn + (size_t)row0 * D_, D_, bp, 64, 3072, 16, smem, acc);
  int tz = threadIdx.x;
  asm volatile("" : "+v"(tz));
  const int l2 = tz & 63, w2i = tz >> 6, fr = l2 & 15, fq = l2 >> 4, wm = w2i >> 2, wn = w2i & 3;
#pragma unroll
  for (int n = 0; n < CfgQKV::NT; ++n) {
    const int col = col0 + wn * 48 + n * 16 + fq * 4;
#pragma unroll
    for (int m = 0; m < CfgQKV::MT; ++m) {
      const int row = row0 + wm * 64 + m * 16 + fr;
      s16x4 o;
#pragma unroll
      for (int r = 0; r < 4; ++r) o[r] = (short)f2bf(acc[m][n][r]);
      *reinterpret_cast<s16x4*>(qkv + (size_t)row * 3072 + col) = o;
    }
  }
}

__global__ __launch_bounds__(512, 2) void gemm1_merged_kernel(const u16* __restrict__ xn, const u16* __restrict__ wqkv,
                                                              const u16* __restrict__ w1, const u16* __restrict__ w3,
                                                              u16* __restrict__ qkv, u16* __restrict__ h) {
  __shared__ __attribute__((aligned(16))) char smem[8 * 16384];
  if (blockIdx.x < 224) gemm_gu8_body(blockIdx.x, smem, xn, w1, w3, h);
  else gemm_qkv_body(blockIdx.x - 224, smem, xn, wqkv, qkv);
}

__global__ __launch_bounds__(256) void attn_kernel(const u16* __restrict__ qkv, u16* __restrict__ o) {
  const int bl = (blockIdx.x & 7) * 128 + (blockIdx.x >> 3);
  const int idx = bl * 256 + threadIdx.x;
  const int c = idx & 3, hh = (idx >> 2) & 31, s = idx >> 7;
  const u16* base = qkv + hh * 32 + c * 8;
  bf16x8 qv = *reinterpret_cast<const bf16x8*>(base + (size_t)s * 3072);
  bf16x8 kv[8], vv[8];
#pragma unroll
  for (int j = 0; j < 8; ++j) {
    const int pos = max(s - 2 * j, 0);
    kv[j] = *reinterpret_cast<const bf16x8*>(base + (size_t)pos * 3072 + 1024);
    vv[j] = *reinterpret_cast<const bf16x8*>(base + (size_t)pos * 3072 + 2048);
  }
  float qf[8];
#pragma unroll
  for (int i = 0; i < 8; ++i) qf[i] = bf2f((u16)qv[i]);
  float lg[8];
  const float scale = 0.17677669529663687f;
#pragma unroll
  for (int j = 0; j < 8; ++j) {
    float d = 0.f;
#pragma unroll
    for (int i = 0; i < 8; ++i) d += qf[i] * bf2f((u16)kv[j][i]);
    d += __shfl_xor(d, 1);
    d += __shfl_xor(d, 2);
    lg[j] = (s - 2 * j >= 0) ? rbf(rbf(d) * scale) : -1e9f;
  }
  float mx = lg[0];
#pragma unroll
  for (int j = 1; j < 8; ++j) mx = fmaxf(mx, lg[j]);
  float e[8], sum = 0.f;
#pragma unroll
  for (int j = 0; j < 8; ++j) { e[j] = __expf(lg[j] - mx); sum += e[j]; }
  const float inv = 1.0f / sum;
  float of[8];
#pragma unroll
  for (int i = 0; i < 8; ++i) of[i] = 0.f;
#pragma unroll
  for (int j = 0; j < 8; ++j) {
    const float a = rbf(e[j] * inv);
#pragma unroll
    for (int i = 0; i < 8; ++i) of[i] += a * bf2f((u16)vv[j][i]);
  }
  bf16x8 ov;
#pragma unroll
  for (int i = 0; i < 8; ++i) ov[i] = (short)f2bf(of[i]);
  *reinterpret_cast<bf16x8*>(o + (size_t)s * D_ + hh * 32 + c * 8) = ov;
}

constexpr int G2_ASZ = 128 * 128;
constexpr int G2_BSZ = 2 * 8192;
constexpr int G2_STAGE = G2_ASZ + G2_BSZ;

__device__ __forceinline__ void gemm2_loop(const u16* __restrict__ A, int lda, const u16* __restrict__ B,
                                           int row0, int col0, int k0, int nkt, char* smem, f32x4 (&acc)[4][4]) {
  const int tid = threadIdx.x, lane = tid & 63, wid = tid >> 6;
  const int wr = wid >> 1, wc = wid & 1;
  const int fr = lane & 15, fq = lane >> 4;
  const u16* asrc[4];
#pragma unroll
  for (int j = 0; j < 4; ++j) {
    const int r = wid * 32 + j * 8 + (lane >> 3);
    const int c = (lane & 7) ^ ((r >> 1) & 7);
    asrc[j] = A + (size_t)(row0 + r) * lda + k0 + c * 8;
  }
  const u16* bsrc[2][2];
#pragma unroll
  for (int j = 0; j < 2; ++j) {
    const int k = wid * 16 + j * 8 + (lane >> 3);
    const int p16 = lane & 7;
    const int g = ((k >> 1) & 1) | (((k >> 3) & 1) << 1);
    const int c = (p16 >> 1) ^ g, half = p16 & 1;
#pragma unroll
    for (int s = 0; s < 2; ++s) bsrc[s][j] = B + (size_t)(k0 + k) * D_ + col0 + s * 64 + c * 16 + half * 8;
  }
  auto stage = [&](int kt, int buf) {
    char* sa = smem + buf * G2_STAGE;
    char* sb = sa + G2_ASZ;
#pragma unroll
    for (int j = 0; j < 4; ++j) glds16(asrc[j] + kt * 64, sa + (wid * 32 + j * 8) * 128 + lane * 16);
#pragma unroll
    for (int s = 0; s < 2; ++s)
#pragma unroll
      for (int j = 0; j < 2; ++j)
        glds16(bsrc[s][j] + (size_t)kt * 64 * D_, sb + s * 8192 + (wid * 16 + j * 8) * 128 + lane * 16);
  };
  const int aswz = (fr >> 1) & 7;
  const int q4 = (lane & 15) >> 2, p4 = lane & 3;
  const int bswz = (q4 >> 1) | ((fq & 1) << 1);

  const unsigned sbase = lds_addr(smem);
  unsigned a_addr[2], b_addr[4];
#pragma unroll
  for (int kk = 0; kk < 2; ++kk) a_addr[kk] = sbase + (wr * 64 + fr) * 128 + (((kk * 4 + fq) ^ aswz) * 16);
#pragma unroll
  for (int n = 0; n < 4; ++n) {
    const int nt = wc * 4 + n;
    b_addr[n] = sbase + G2_ASZ + (nt >> 2) * 8192 + (fq * 8 + q4) * 128 + (((nt & 3) ^ bswz) * 32) + p4 * 8;
  }
  stage(0, 0);
  asm volatile("s_waitcnt vmcnt(0)" ::: "memory");
  __syncthreads();
  for (int kt = 0; kt < nkt; ++kt) {
    const int buf = kt & 1;
    if (kt + 1 < nkt) stage(kt + 1, buf ^ 1);
    const unsigned so = buf * G2_STAGE;
#pragma unroll
    for (int kk = 0; kk < 2; ++kk) {
      s16x4 bl[4], bh[4];
      bf16x8 a[4];
#pragma unroll
      for (int n = 0; n < 4; ++n) {
        if (kk == 0) lds_tr_issue<0>(bl[n], bh[n], b_addr[n] + so);
        else lds_tr_issue<4096>(bl[n], bh[n], b_addr[n] + so);
      }
      const unsigned aa = a_addr[kk] + so;
      lds_b128_issue<0 * 2048>(a[0], aa);
      lds_b128_issue<1 * 2048>(a[1], aa);
      lds_b128_issue<2 * 2048>(a[2], aa);
      lds_b128_issue<3 * 2048>(a[3], aa);
      asm volatile("s_waitcnt lgkmcnt(0)"
                   : "+v"(bl[0]), "+v"(bh[0]), "+v"(bl[1]), "+v"(bh[1]), "+v"(bl[2]), "+v"(bh[2]), "+v"(bl[3]),
                     "+v"(bh[3]), "+v"(a[0]), "+v"(a[1]), "+v"(a[2]), "+v"(a[3])::"memory");
#pragma unroll
      for (int m = 0; m < 4; ++m)
#pragma unroll
        for (int n = 0; n < 4; ++n)
          acc[m][n] = __builtin_amdgcn_mfma_f32_16x16x32_bf16(cat4(bl[n], bh[n]), a[m], acc[m][n], 0, 0, 0);
    }
    asm volatile("s_waitcnt vmcnt(0)" ::: "memory");
    __syncthreads();
  }
}

typedef RingCfg<128, 128, 2, 2, 4> CfgG2;
__global__ __launch_bounds__(256, 1) void gemm2_kernel(const u16* __restrict__ o, const u16* __restrict__ wo,
                                                       const u16* __restrict__ h, const u16* __restrict__ w2,
                                                       u16* __restrict__ sbuf, float* __restrict__ g0,
                                                       float* __restrict__ g1) {
  __shared__ __attribute__((aligned(16))) char smem[CfgG2::LDS];
  const int tid = threadIdx.x, lane = tid & 63, wid = tid >> 6;
  const int wr = wid >> 1, wc = wid & 1;
  const int fr = lane & 15, fq = lane >> 4;
  const int xcd = blockIdx.x & 7, idx = blockIdx.x >> 3;
  const int split = idx & 1;
  const int t = idx >> 1;
  const int tn = (xcd & 1) * 4 + (t & 3);
  const int tm = (xcd >> 1) * 4 + (t >> 2);
  const int row0 = tm * 128, col0 = tn * 128;
  const int krow = wid * 16 + (lane >> 3);
  const int g = ((krow >> 1) & 1) | (((krow >> 3) & 1) << 1);
  const int lc = (((lane & 7) >> 1) ^ g) * 16 + (lane & 1) * 8;
  f32x4 acc[4][4];
#pragma unroll
  for (int m = 0; m < 4; ++m)
#pragma unroll
    for (int n = 0; n < 4; ++n) acc[m][n] = (f32x4){0.f, 0.f, 0.f, 0.f};
  if (split == 0) {
    gemm_ring<CfgG2>(o + (size_t)row0 * D_, D_, wo + (size_t)krow * D_ + col0 + lc, 64, D_, 16, smem, acc, (lc ^ 32) - lc);
#pragma unroll
    for (int m = 0; m < 4; ++m)
#pragma unroll
      for (int n = 0; n < 4; ++n) {
        const int row = row0 + wr * 64 + m * 16 + fr, col = col0 + wc * 64 + n * 16 + fq * 4;
        s16x4 ov;
#pragma unroll
        for (int r = 0; r < 4; ++r) ov[r] = (short)f2bf(acc[m][n][r]);
        *reinterpret_cast<s16x4*>(sbuf + (size_t)row * D_ + col) = ov;
        acc[m][n] = (f32x4){0.f, 0.f, 0.f, 0.f};
      }
    gemm_ring<CfgG2>(h + (size_t)row0 * F_, F_, w2 + (size_t)krow * D_ + col0 + lc, 64, D_, 20, smem, acc, (lc ^ 32) - lc);
  } else {
    gemm_ring<CfgG2>(h + (size_t)row0 * F_ + 1280, F_, w2 + (size_t)(1280 + krow) * D_ + col0 + lc, 64, D_, 36, smem, acc, (lc ^ 32) - lc);
  }
  float* gd = split == 0 ? g0 : g1;
#pragma unroll
  for (int m = 0; m < 4; ++m)
#pragma unroll
    for (int n = 0; n < 4; ++n) {
      const int row = row0 + wr * 64 + m * 16 + fr, col = col0 + wc * 64 + n * 16 + fq * 4;
      *reinterpret_cast<f32x4*>(gd + (size_t)row * D_ + col) = acc[m][n];
    }
}

__global__ __launch_bounds__(256) void final_kernel(const u16* __restrict__ x, const u16* __restrict__ sbuf,
                                                    const float* __restrict__ g0, const float* __restrict__ g1,
                                                    u16* __restrict__ out) {
  const int xc = blockIdx.x & 7, bi = blockIdx.x >> 3;
  const int e = bi * 256 + threadIdx.x;
  const size_t i = (size_t)((xc >> 1) * 512 + (e >> 6)) * D_ + (xc & 1) * 512 + (e & 63) * 8;
  bf16x8 xv = *reinterpret_cast<const bf16x8*>(x + i);
  bf16x8 sv = *reinterpret_cast<const bf16x8*>(sbuf + i);
  f32x4 a0 = *reinterpret_cast<const f32x4*>(g0 + i), a1 = *reinterpret_cast<const f32x4*>(g0 + i + 4);
  f32x4 b0 = *reinterpret_cast<const f32x4*>(g1 + i), b1 = *reinterpret_cast<const f32x4*>(g1 + i + 4);
  bf16x8 ov;
#pragma unroll
  for (int k = 0; k < 8; ++k) {
    const float g = rbf(k < 4 ? a0[k] + b0[k] : a1[k - 4] + b1[k - 4]);
    const float sg = rbf(silu_f(g));
    const float p = rbf(bf2f((u16)sv[k]) * sg);
    ov[k] = (short)f2bf(bf2f((u16)xv[k]) + p);
  }
  *reinterpret_cast<bf16x8*>(out + i) = ov;
}

extern "C" void kernel_launch(void* const* d_in, const int* in_sizes, int n_in, void* d_out, int out_size,
                              void* d_ws, size_t ws_size, hipStream_t stream) {
  const u16* x = (const u16*)d_in[0];
  const u16* wqkv = (const u16*)d_in[1];
  const u16* wo = (const u16*)d_in[2];
  const u16* w1 = (const u16*)d_in[4];
  const u16* w3 = (const u16*)d_in[5];
  const u16* w2 = (const u16*)d_in[6];
  u16* out = (u16*)d_out;
  char* ws = (char*)d_ws;
  u16* xn = (u16*)ws;
  u16* qkv = (u16*)(ws + (4u << 20));
  u16* h = (u16*)(ws + (16u << 20));
  u16* o = (u16*)(ws + (32u << 20));
  u16* sbuf = (u16*)(ws + (36u << 20));
  float* g0 = (float*)(ws + (40u << 20));
  float* g1 = (float*)(ws + (48u << 20));

  ln_kernel<<<S_ / 4, 256, 0, stream>>>(x, xn);
  gemm1_merged_kernel<<<480, 512, 0, stream>>>(xn, wqkv, w1, w3, qkv, h);
  attn_kernel<<<S_ * NH * 4 / 256, 256, 0, stream>>>(qkv, o);
  gemm2_kernel<<<256, 256, 0, stream>>>(o, wo, h, w2, sbuf, g0, g1);
  final_kernel<<<S_ * D_ / 8 / 256, 256, 0, stream>>>(x, sbuf, g0, g1, out);
}
```
